```python
import torch

def _softmax(x, axis=-1):
    return torch.softmax(x, dim=axis)
def _gelu(x):
    return torch.nn.functional.gelu(x, approximate="tanh")
def _silu(x):
    return torch.nn.functional.silu(x)
def _sigmoid(x):
    return torch.sigmoid(x)
def _layernorm(x, axis=-1, eps=1e-5):
    m = x.mean(dim=axis, keepdim=True)
    v = x.var(dim=axis, keepdim=True, unbiased=False)
    return (x - m) / torch.sqrt(v + eps)
def _scatter_add(tgt, idx, src, axis=0):
    return tgt.clone().index_add_(axis, idx, src)
def _segment_sum(src, seg, n):
    out = torch.zeros((n,) + src.shape[1:], dtype=src.dtype)
    return out.index_add_(0, seg, src)
def _pad(x, pw):
    flat = tuple(v for pair in reversed(pw) for v in pair)
    return torch.nn.functional.pad(x, flat)

kernel_name = 'standalone__paged_block_table_kv_pagedattention__sampling'

def setup_inputs():

    _w = (lambda a: (lambda t: t.to(torch.bfloat16) if t.is_floating_point() else t)(torch.as_tensor(a)))
    B, Hq, Hkv, Sq, Skv, D = 32, 16, 8, 1, 2048, 256
    bs = 16
    nbps = max(1, Skv // bs)
    num_phys = B * nbps + 4
    bt = torch.randperm(num_phys)[: B * nbps].reshape(B, nbps).to(torch.int64)
    cl = (nbps * bs - (torch.arange(1, B + 1, dtype=torch.int64) * 2654435761) % max(1, nbps * bs // 2)).to(torch.int64)
    return {
        "q": _w((lambda *s: torch.randn(*s, dtype=torch.bfloat16) * 0.8)(B, Hq, Sq, D)),
        "k_cache": _w((lambda *s: torch.randn(*s, dtype=torch.bfloat16) * 0.8)(num_phys, bs, Hkv, D)),
        "v_cache": _w((lambda *s: torch.randn(*s, dtype=torch.bfloat16) * 0.05)(num_phys, bs, Hkv, D)),
        "block_table": _w(bt),
        "context_lens": _w(cl),
    }

def reference(q, k_cache, v_cache, block_table, context_lens):
    B, Hq, Sq, D = q.shape
    bs = k_cache.shape[1]
    Hkv = k_cache.shape[2]
    nbps = block_table.shape[1]
    Skv = nbps * bs
    bt = block_table.reshape(-1)
    k = (lambda x, *d: x.permute(*d))(k_cache[bt].reshape(B, Skv, Hkv, D), 0, 2, 1, 3)
    v = (lambda x, *d: x.permute(*d))(v_cache[bt].reshape(B, Skv, Hkv, D), 0, 2, 1, 3)
    g = Hq // Hkv
    qg = q.reshape(B, Hkv, g, Sq, D)
    scores = torch.einsum("bhgqd,bhkd->bhgqk", qg, k) * (D ** -0.5)
    pos = torch.arange(Skv)
    msk = pos[None, :] < context_lens[:, None]
    scores = torch.where(msk[:, None, None, None, :], scores, -1e9)
    p = _softmax(scores, axis=-1)
    out = torch.einsum("bhgqk,bhkd->bhgqd", p, v)
    return out.reshape(B, Hq, Sq, D)
```

```cpp
#include <hip/hip_runtime.h>
#include <hip/hip_bf16.h>
#include <cstdint>

constexpr int B = 32, HQ = 16, HKV = 8, D = 256, PAGE = 16, NBPS = 128, G = 2;
constexpr int NSPLIT = 2, WAVES = 4, NTHR = WAVES * 64;
constexpr int TOK_STRIDE = HKV * D;
constexpr int PAGE_ELEMS = PAGE * HKV * D;

using bf16x2 = __attribute__((ext_vector_type(2))) __bf16;
using u32x4  = __attribute__((ext_vector_type(4))) unsigned;
using f32x4  = __attribute__((ext_vector_type(4))) float;

__device__ __forceinline__ float dot2(unsigned k, unsigned q, float acc) {
  return __builtin_amdgcn_fdot2_f32_bf16(__builtin_bit_cast(bf16x2, k), __builtin_bit_cast(bf16x2, q), acc, false);
}
template <int CTRL> __device__ __forceinline__ float dpp(float x) {
  return __builtin_bit_cast(float, __builtin_amdgcn_mov_dpp(__builtin_bit_cast(int, x), CTRL, 0xf, 0xf, true));
}
constexpr int XOR1 = 0xB1, XOR2 = 0x4E, XOR7 = 0x141, XOR8 = 0x128, ROR4 = 0x124;

__device__ __forceinline__ float sum16swap(float a, float b) {
  auto s = __builtin_amdgcn_permlane16_swap(__float_as_uint(a), __float_as_uint(b), false, false);
  return __uint_as_float(s[0]) + __uint_as_float(s[1]);
}
__device__ __forceinline__ float max16(float a) {
  auto s = __builtin_amdgcn_permlane16_swap(__float_as_uint(a), __float_as_uint(a), false, false);
  return fmaxf(__uint_as_float(s[0]), __uint_as_float(s[1]));
}
__device__ __forceinline__ float add16(float a) {
  auto s = __builtin_amdgcn_permlane16_swap(__float_as_uint(a), __float_as_uint(a), false, false);
  return __uint_as_float(s[0]) + __uint_as_float(s[1]);
}
__device__ __forceinline__ float max32(float a) {
  auto s = __builtin_amdgcn_permlane32_swap(__float_as_uint(a), __float_as_uint(a), false, false);
  return fmaxf(__uint_as_float(s[0]), __uint_as_float(s[1]));
}
__device__ __forceinline__ float add32(float a) {
  auto s = __builtin_amdgcn_permlane32_swap(__float_as_uint(a), __float_as_uint(a), false, false);
  return __uint_as_float(s[0]) + __uint_as_float(s[1]);
}

constexpr int MRG_REC = 4 + D;
constexpr int LDS_P = 0, LDS_MRG = WAVES * 64, LDS_FLOATS = LDS_MRG + WAVES * G * MRG_REC;

__global__ void __launch_bounds__(NTHR) attn_split(const uint16_t* __restrict__ q, const uint16_t* __restrict__ kc,
                                                   const uint16_t* __restrict__ vc, const int* __restrict__ bt,
                                                   const int* __restrict__ lens, float* __restrict__ pml,
                                                   float* __restrict__ po) {
  __shared__ __attribute__((aligned(16))) float lds[LDS_FLOATS];
  const int s = blockIdx.x, kvh = blockIdx.y, b = blockIdx.z;
  const int tid = threadIdx.x, lane = tid & 63, wave = __builtin_amdgcn_readfirstlane(tid >> 6), r = lane >> 5, c = lane & 31;
  constexpr float C2 = 0.0625f * 1.4426950408889634f;
  int len = lens[b];
  const bool uni = len <= 0;
  len = uni ? NBPS * PAGE : min(len, NBPS * PAGE);
  const int npages = (len + PAGE - 1) / PAGE;
  const int pps = (npages + NSPLIT - 1) / NSPLIT;
  const int p0 = s * pps, p1 = min(p0 + pps, npages);
  const int nwp = (p1 > p0 + wave) ? (p1 - p0 - wave + WAVES - 1) / WAVES : 0;
  int btv = 0;
  { const int pg = min(p0 + wave + WAVES * lane, NBPS - 1); btv = bt[b * NBPS + pg]; }
  u32x4 qpk[G];
#pragma unroll
  for (int g = 0; g < G; ++g) {
    qpk[g] = *reinterpret_cast<const u32x4*>(q + ((size_t)(b * HQ + kvh * G + g)) * D + 8 * c);
    if (uni) qpk[g] = u32x4{0u, 0u, 0u, 0u};
  }
  const size_t loff = (size_t)r * TOK_STRIDE + 8 * c + kvh * D;
  u32x4 Kn[8], Vn[8];
#define ISSUE(i_) do { const int phys_ = __builtin_amdgcn_readlane(btv, (i_)); \
    const uint16_t* kb_ = kc + (size_t)phys_ * PAGE_ELEMS + loff; const uint16_t* vb_ = vc + (size_t)phys_ * PAGE_ELEMS + loff; \
    _Pragma("unroll") for (int t_ = 0; t_ < 8; ++t_) Kn[t_] = __builtin_nontemporal_load(reinterpret_cast<const u32x4*>(kb_ + (size_t)t_ * 2 * TOK_STRIDE)); \
    _Pragma("unroll") for (int t_ = 0; t_ < 8; ++t_) Vn[t_] = __builtin_nontemporal_load(reinterpret_cast<const u32x4*>(vb_ + (size_t)t_ * 2 * TOK_STRIDE)); } while (0)
  float m = -INFINITY, l = 0.f, acc[G * 8];
#pragma unroll
  for (int k = 0; k < G * 8; ++k) acc[k] = 0.f;
  if (nwp > 0) ISSUE(0);
  for (int i = 0; i < nwp; ++i) {
    u32x4 Kc[8], Vc[8];
#pragma unroll
    for (int t = 0; t < 8; ++t) { Kc[t] = Kn[t]; Vc[t] = Vn[t]; }
    const int pg = p0 + wave + WAVES * i, rem = len - pg * PAGE;
    if (i + 1 < nwp) ISSUE(i + 1);
    float x[16];
#pragma unroll
    for (int t = 0; t < 8; ++t)
#pragma unroll
      for (int g = 0; g < G; ++g) {
        float d = 0.f;
#pragma unroll
        for (int k = 0; k < 4; ++k) d = dot2(Kc[t][k], qpk[g][k], d);
        x[t * 2 + g] = d;
      }
#pragma unroll
    for (int h = 0; h < 8; ++h) x[h] = sum16swap(x[h], x[h + 8]);
#define TR_STEP(HALF, CTRL, BIT) _Pragma("unroll") for (int h = 0; h < (HALF); ++h) { \
      const float lo_ = x[h], hi_ = x[h + (HALF)]; const float t1_ = lo_ + dpp<CTRL>(lo_), t2_ = hi_ + dpp<CTRL>(hi_); \
      x[h] = (c & (BIT)) ? t2_ : t1_; }
    TR_STEP(4, XOR8, 8) TR_STEP(2, XOR7, 4) TR_STEP(1, XOR2, 2)
#undef TR_STEP
    float sc = x[0] + dpp<XOR1>(x[0]);
    const bool tail = rem < PAGE;
    const bool live = !tail || (2 * (c >> 2) + r) < rem;
    sc = live ? sc * C2 : -INFINITY;
    float pm = sc; pm = fmaxf(pm, dpp<ROR4>(pm)); pm = fmaxf(pm, dpp<XOR8>(pm)); pm = max16(pm); pm = max32(pm);
    const float mn = fmaxf(m, pm), msub = (mn == -INFINITY) ? 0.f : mn;
    const float alpha = __builtin_amdgcn_exp2f(m - msub), p = __builtin_amdgcn_exp2f(sc - msub);
    l = fmaf(l, alpha, p); m = mn;
    const float a0 = dpp<0x00>(alpha), a1 = dpp<0xAA>(alpha);
#pragma unroll
    for (int k = 0; k < 8; ++k) { acc[k] *= a0; acc[8 + k] *= a1; }
    float* prow = lds + LDS_P + wave * 64 + r * 32;
    if ((c & 1) == 0) prow[c >> 1] = p;
    asm volatile("s_waitcnt lgkmcnt(0)" ::: "memory");
    f32x4 pr[4];
#pragma unroll
    for (int u = 0; u < 4; ++u) pr[u] = *reinterpret_cast<const f32x4*>(prow + 4 * u);
    if (tail) {
#pragma unroll
      for (int t = 0; t < 8; ++t) { const bool lv = (2 * t + r) < rem;
#pragma unroll
        for (int k = 0; k < 4; ++k) Vc[t][k] = lv ? Vc[t][k] : 0u; } }
#pragma unroll
    for (int t = 0; t < 8; ++t) {
      float vf[8];
#pragma unroll
      for (int k = 0; k < 4; ++k) { vf[2 * k] = __builtin_bit_cast(float, Vc[t][k] << 16); vf[2 * k + 1] = __builtin_bit_cast(float, Vc[t][k] & 0xffff0000u); }
#pragma unroll
      for (int g = 0; g < G; ++g) { const float pg_ = pr[(t * 2 + g) >> 2][(t * 2 + g) & 3];
#pragma unroll
        for (int k = 0; k < 8; ++k) acc[g * 8 + k] = fmaf(pg_, vf[k], acc[g * 8 + k]); } }
  }
#undef ISSUE
#pragma unroll
  for (int k = 0; k < G * 8; ++k) acc[k] = add32(acc[k]);
  { float t = l; t += dpp<ROR4>(t); t += dpp<XOR8>(t); t = add16(t); l = add32(t); }
  float* mrg = lds + LDS_MRG;
  if (r == 0) {
#pragma unroll
    for (int g = 0; g < G; ++g) {
      float* rec = mrg + (wave * G + g) * MRG_REC;
#pragma unroll
      for (int k = 0; k < 8; k += 4) *reinterpret_cast<f32x4*>(rec + 4 + 8 * c + k) = f32x4{acc[g * 8 + k], acc[g * 8 + k + 1], acc[g * 8 + k + 2], acc[g * 8 + k + 3]};
      if (c == 2 * g) { rec[0] = m; rec[1] = l; } } }
  __syncthreads();
  {
    const int g = tid >> 7, d0 = 2 * (tid & 127);
    const size_t row = (size_t)b * HQ + kvh * G + g;
    float M = -INFINITY;
#pragma unroll
    for (int w = 0; w < WAVES; ++w) M = fmaxf(M, mrg[(w * G + g) * MRG_REC]);
    float L = 0.f, o0 = 0.f, o1 = 0.f;
#pragma unroll
    for (int w = 0; w < WAVES; ++w) { const float* rec = mrg + (w * G + g) * MRG_REC; const float lw = rec[1];
      const float wt = (lw > 0.f) ? __builtin_amdgcn_exp2f(rec[0] - M) : 0.f;
      L = fmaf(wt, lw, L); o0 = fmaf(wt, rec[4 + d0], o0); o1 = fmaf(wt, rec[4 + d0 + 1], o1); }
    const size_t slot = row * NSPLIT + s;
    *reinterpret_cast<float2*>(po + slot * D + d0) = make_float2(o0, o1);
    if ((tid & 127) == 0) *reinterpret_cast<float2*>(pml + 2 * slot) = make_float2(M, L);
  }
}

__global__ void __launch_bounds__(128) attn_combine(const float* __restrict__ pml, const float* __restrict__ po, __hip_bfloat16* __restrict__ out) {
  const size_t row = blockIdx.x;
  const int d0 = 2 * threadIdx.x;
  const float* pm = pml + row * NSPLIT * 2;
  const float* pp = po + row * NSPLIT * D;
  float M = -INFINITY;
#pragma unroll
  for (int s = 0; s < NSPLIT; ++s) M = fmaxf(M, pm[2 * s]);
  float L = 0.f, o0 = 0.f, o1 = 0.f;
#pragma unroll
  for (int s = 0; s < NSPLIT; ++s) {
    const float ls = pm[2 * s + 1], w = (ls > 0.f) ? __builtin_amdgcn_exp2f(pm[2 * s] - M) : 0.f;
    const float2 a = *reinterpret_cast<const float2*>(pp + (size_t)s * D + d0);
    L = fmaf(w, ls, L); o0 = fmaf(w, a.x, o0); o1 = fmaf(w, a.y, o1);
  }
  const float inv = (L > 0.f) ? 1.f / L : 0.f;
  __hip_bfloat162 o; o.x = __float2bfloat16(o0 * inv); o.y = __float2bfloat16(o1 * inv);
  *reinterpret_cast<__hip_bfloat162*>(out + row * D + d0) = o;
}

extern "C" void kernel_launch(void* const* d_in, const int* in_sizes, int n_in, void* d_out, int out_size, void* d_ws,
                              size_t ws_size, hipStream_t stream) {
  const uint16_t* q = (const uint16_t*)d_in[0];
  const uint16_t* k = (const uint16_t*)d_in[1];
  const uint16_t* v = (const uint16_t*)d_in[2];
  const int* bt = (const int*)d_in[3];
  const int* lens = (const int*)d_in[4];
  float* pml = (float*)d_ws;
  float* po = (float*)((char*)d_ws + 65536);
  attn_split<<<dim3(NSPLIT, HKV, B), NTHR, 0, stream>>>(q, k, v, bt, lens, pml, po);
  attn_combine<<<B * HQ, 128, 0, stream>>>(pml, po, (__hip_bfloat16*)d_out);
}
```

```cpp
#include <hip/hip_runtime.h>
#include <hip/hip_bf16.h>
#include <cstdint>

#define CFG_W 4
#define CFG_UNIT 16
#define CFG_DEPTH 2

constexpr int B = 32, HQ = 16, HKV = 8, D = 256, PAGE = 16, NBPS = 128, SKV = NBPS * PAGE;
constexpr int W = CFG_W, UNIT = CFG_UNIT, DEPTH = CFG_DEPTH;
constexpr int UPP = PAGE / UNIT;
constexpr int NL = UNIT / 2;
constexpr int NU = SKV / UNIT;
constexpr int R0 = W * DEPTH;
constexpr int NPOS = ((NU + 2 * R0 + 63) / 64) * 64;
constexpr int NSLOT = 2;
constexpr int REC = 8 + 2 * D;
constexpr int NREC = 2 * W + NSLOT * W + 1;
constexpr int NTHR = (W + 1) * 64;

constexpr int L_CTL = 0;
constexpr int L_LIST = 256;
constexpr int L_Q = L_LIST + NPOS * 8;
constexpr int L_REC = L_Q + 33 * 1024;
constexpr int L_END = L_REC + NREC * REC * 4;
constexpr int C_READY = 0, C_NTOT = 1, C_SVC = 2, C_PROG = 8;

constexpr size_t WS_EPOCH = 0;
constexpr size_t WS_FLAG = 4096;
constexpr size_t WS_PAY = 1 << 20;
constexpr int PAY_STRIDE = 4096;

using bf16x2 = __attribute__((ext_vector_type(2))) __bf16;
using u32x4 = __attribute__((ext_vector_type(4))) unsigned;
using f32x4 = __attribute__((ext_vector_type(4))) float;
using f32x2 = __attribute__((ext_vector_type(2))) float;
using i32x2 = __attribute__((ext_vector_type(2))) int;
typedef unsigned long long u64;
template <int N> struct IC { static constexpr int value = N; };
#define GAS __attribute__((address_space(1)))
#define LAS __attribute__((address_space(3)))
#define RLX_AGENT __ATOMIC_RELAXED, __HIP_MEMORY_SCOPE_AGENT
#define RLX_WG __ATOMIC_RELAXED, __HIP_MEMORY_SCOPE_WORKGROUP

__device__ __forceinline__ float dot2(unsigned k, unsigned q, float acc) {
  return __builtin_amdgcn_fdot2_f32_bf16(__builtin_bit_cast(bf16x2, k), __builtin_bit_cast(bf16x2, q), acc, false);
}
template <int CTRL> __device__ __forceinline__ float dpp(float x) {
  return __builtin_bit_cast(float, __builtin_amdgcn_mov_dpp(__builtin_bit_cast(int, x), CTRL, 0xf, 0xf, true));
}
template <int CTRL> __device__ __forceinline__ int dppi(int x) {
  return __builtin_amdgcn_update_dpp(0, x, CTRL, 0xf, 0xf, true);
}
constexpr int XOR1 = 0xB1, XOR2 = 0x4E, XOR7 = 0x141, XOR8 = 0x128, ROR4 = 0x124, NEWBC = 0x150;

__device__ __forceinline__ float sum16swap(float a, float b) {
  auto s = __builtin_amdgcn_permlane16_swap(__float_as_uint(a), __float_as_uint(b), false, false);
  return __uint_as_float(s[0]) + __uint_as_float(s[1]);
}
__device__ __forceinline__ float sum32swap(float a, float b) {
  auto s = __builtin_amdgcn_permlane32_swap(__float_as_uint(a), __float_as_uint(b), false, false);
  return __uint_as_float(s[0]) + __uint_as_float(s[1]);
}
__device__ __forceinline__ float max16(float a) {
  auto s = __builtin_amdgcn_permlane16_swap(__float_as_uint(a), __float_as_uint(a), false, false);
  return fmaxf(__uint_as_float(s[0]), __uint_as_float(s[1]));
}
__device__ __forceinline__ float max32(float a) {
  auto s = __builtin_amdgcn_permlane32_swap(__float_as_uint(a), __float_as_uint(a), false, false);
  return fmaxf(__uint_as_float(s[0]), __uint_as_float(s[1]));
}
__device__ __forceinline__ int lds_ld(LAS int* p) { return __hip_atomic_load(p, RLX_WG); }
__device__ __forceinline__ void lds_st(LAS int* p, int v) { __hip_atomic_store(p, v, RLX_WG); }
#define LGKM0() asm volatile("s_waitcnt lgkmcnt(0)" ::: "memory")
#define VM0() asm volatile("s_waitcnt vmcnt(0)" ::: "memory")

__device__ __forceinline__ int scan32(int x) {
  x += dppi<0x111>(x); x += dppi<0x112>(x); x += dppi<0x114>(x); x += dppi<0x118>(x);
  const int r0 = __builtin_amdgcn_readlane(x, 15);
  return x + (((threadIdx.x & 63) >= 16) ? r0 : 0);
}

__global__ void __launch_bounds__(NTHR, (W + 4) / 4) attn_kernel(const uint16_t* __restrict__ q, const char* __restrict__ kc,
                                                    const char* __restrict__ vc, const int* __restrict__ bt,
                                                    const int* __restrict__ lens, uint16_t* __restrict__ out,
                                                    char* __restrict__ ws) {
  __shared__ __attribute__((aligned(16))) char smem[L_END];
  LAS char* sm = (LAS char*)smem;
  LAS int* ctl = (LAS int*)(sm + L_CTL);
  LAS float* recs = (LAS float*)(sm + L_REC);
  const int b = blockIdx.x >> 3, h = blockIdx.x & 7;
  const int tid = threadIdx.x, lane = tid & 63, wave = __builtin_amdgcn_readfirstlane(tid >> 6);
  const int r = lane >> 5, c = lane & 31;
  constexpr float C2 = 0.0625f * 1.4426950408889634f;

  if (wave < W) {
    const int w = wave;
    const int len_raw = lens[b];
    int ph0[DEPTH];
#pragma unroll
    for (int k = 0; k < DEPTH; ++k) { const int u = w + W * k; ph0[k] = bt[b * NBPS + u / UPP] * UPP + (u % UPP); }
    u32x4 qv[2];
#pragma unroll
    for (int g = 0; g < 2; ++g) qv[g] = *reinterpret_cast<const u32x4*>(q + (size_t)(b * HQ + h * 2 + g) * D + 8 * c);
    const bool uni = len_raw <= 0;
    const int len = uni ? SKV : min(len_raw, SKV);
    const int voff = r * 4096 + h * 512 + c * 16;
    u32x4 K[DEPTH][NL], V[DEPTH][NL];
    int meta[DEPTH];
#define ISSUE(BUF, base_, s_, phys_, rem_) do { \
      const __amdgpu_buffer_rsrc_t rs_ = __builtin_amdgcn_make_buffer_rsrc((void*)((base_) + (size_t)(unsigned)(phys_) * (UNIT * 4096)), 0, (rem_) * 4096, 0x00020000); \
      _Pragma("unroll") for (int t_ = 0; t_ < NL; ++t_) BUF[s_][t_] = __builtin_amdgcn_raw_buffer_load_b128(rs_, voff, t_ * 8192, 2); } while (0)
#pragma unroll
    for (int k = 0; k < DEPTH; ++k) {
      const int u = w + W * k;
      const int rem = __builtin_amdgcn_readfirstlane(max(0, min(UNIT, len - u * UNIT)));
      meta[k] = rem;
      ISSUE(K, kc, k, ph0[k], rem);
      ISSUE(V, vc, k, ph0[k], rem);
    }
    if (lane < 2 + NSLOT) {
      const int ri = lane < 2 ? lane * W + w : 2 * W + (lane - 2) * W + w;
      LAS float* rec = recs + ri * REC;
      rec[0] = -INFINITY; rec[1] = 0.f; rec[2] = -INFINITY; rec[3] = 0.f; ((LAS int*)rec)[4] = -1;
    }
    if (lane == 0) lds_st(ctl + C_PROG + w, 0);
    LGKM0();
    __builtin_amdgcn_s_barrier();
    if (uni) { qv[0] = u32x4{0u, 0u, 0u, 0u}; qv[1] = qv[0]; }

    float m = -INFINITY, l = 0.f, acc[16];
#pragma unroll
    for (int k = 0; k < 16; ++k) acc[k] = 0.f;
    int cur = 0;
    while (lds_ld(ctl + C_READY) == 0) __builtin_amdgcn_s_sleep(1);
    asm volatile("" ::: "memory");
    const int ntot = __builtin_amdgcn_readfirstlane(lds_ld(ctl + C_NTOT));
    const int cnt = (ntot - w + W - 1) / W;

    auto dump = [&](int seg, int next) __attribute__((always_inline)) {
      int ri;
      if (seg == 0) ri = w;
      else if (seg == 255) ri = W + w;
      else {
        ri = 2 * W + ((seg - 1) % NSLOT) * W + w;
        while (lds_ld(ctl + C_SVC) < seg - NSLOT) __builtin_amdgcn_s_sleep(1);
        asm volatile("" ::: "memory");
      }
      LAS float* rec = recs + ri * REC;
      float o[8];
#pragma unroll
      for (int k = 0; k < 8; ++k) o[k] = sum32swap(acc[k], acc[8 + k]);
      float t = l;
      if constexpr (UNIT == 16) t += dpp<ROR4>(t);
      t += dpp<XOR8>(t); t = sum16swap(t, t); t = sum32swap(t, t);
      *(LAS f32x4*)(rec + 8 + lane * 8) = f32x4{o[0], o[1], o[2], o[3]};
      *(LAS f32x4*)(rec + 8 + lane * 8 + 4) = f32x4{o[4], o[5], o[6], o[7]};
      constexpr int G1 = UNIT == 16 ? 2 : 4;
      if (lane == 0 || lane == G1) *(LAS f32x2*)(rec + (lane ? 2 : 0)) = f32x2{m, t};
      if (lane == 0) ((LAS int*)rec)[4] = seg;
      LGKM0();
      if (lane == 0) lds_st(ctl + C_PROG + w, next);
    };

    int i = 0;
    auto stage = [&](auto sc_) __attribute__((always_inline)) {
      {
        constexpr int s = decltype(sc_)::value;
        const i32x2 ent = *(LAS i32x2*)(sm + L_LIST + (w + W * (i + DEPTH)) * 8);
        const int rem = meta[s] & 255, seg = meta[s] >> 8;
        if (seg != cur) {
          dump(cur, seg);
          m = -INFINITY; l = 0.f;
#pragma unroll
          for (int k = 0; k < 16; ++k) acc[k] = 0.f;
          const int qi = seg == 255 ? 0 : seg;
          qv[0] = *(LAS u32x4*)(sm + L_Q + qi * 1024 + c * 16);
          qv[1] = *(LAS u32x4*)(sm + L_Q + qi * 1024 + 512 + c * 16);
          cur = seg;
        }
        float x[2 * NL];
#pragma unroll
        for (int t = 0; t < NL; ++t)
#pragma unroll
          for (int g = 0; g < 2; ++g) {
            float d = 0.f;
#pragma unroll
            for (int k = 0; k < 4; ++k) d = dot2(K[s][t][k], qv[g][k], d);
            x[t * 2 + g] = d;
          }
        const int nphys = __builtin_amdgcn_readfirstlane(ent.x), nmeta = __builtin_amdgcn_readfirstlane(ent.y);
        ISSUE(K, kc, s, nphys, nmeta & 255);
#pragma unroll
        for (int hh = 0; hh < NL; ++hh) x[hh] = sum16swap(x[hh], x[hh + NL]);
#define TR_STEP(HALF, CTRL, BIT) _Pragma("unroll") for (int hh = 0; hh < (HALF); ++hh) { \
          const float lo_ = x[hh], hi_ = x[hh + (HALF)]; const float t1_ = lo_ + dpp<CTRL>(lo_), t2_ = hi_ + dpp<CTRL>(hi_); \
          x[hh] = (c & (BIT)) ? t2_ : t1_; }
        float sc; int tok;
        if constexpr (UNIT == 16) {
          TR_STEP(4, XOR8, 8) TR_STEP(2, XOR7, 4) TR_STEP(1, XOR2, 2)
          sc = x[0] + dpp<XOR1>(x[0]);
          tok = 2 * (c >> 2) + r;
        } else {
          TR_STEP(2, XOR8, 8) TR_STEP(1, XOR7, 4)
          sc = x[0] + dpp<XOR2>(x[0]);
          sc = sc + dpp<XOR1>(sc);
          tok = 2 * (c >> 3) + r;
        }
#undef TR_STEP
        sc = (tok < rem) ? sc * C2 : -INFINITY;
        float pm = sc;
        if constexpr (UNIT == 16) pm = fmaxf(pm, dpp<ROR4>(pm));
        pm = fmaxf(pm, dpp<XOR8>(pm)); pm = max16(pm); pm = max32(pm);
        const float mn = fmaxf(m, pm), msub = (mn == -INFINITY) ? 0.f : mn;
        const float alpha = __builtin_amdgcn_exp2f(m - msub), p = __builtin_amdgcn_exp2f(sc - msub);
        l = fmaf(l, alpha, p); m = mn;
        const float a0 = __builtin_bit_cast(float, __builtin_amdgcn_readlane(__builtin_bit_cast(int, alpha), 0));
        const float a1 = __builtin_bit_cast(float, __builtin_amdgcn_readlane(__builtin_bit_cast(int, alpha), UNIT == 16 ? 2 : 4));
        if (__builtin_bit_cast(int, a0) != 0x3f800000 || __builtin_bit_cast(int, a1) != 0x3f800000) {
#pragma unroll
          for (int k = 0; k < 8; ++k) { acc[k] *= a0; acc[8 + k] *= a1; }
        }
        auto ps = __builtin_amdgcn_permlane16_swap(__float_as_uint(p), __float_as_uint(p), false, false);
        const float pl = __uint_as_float(ps[0]), phh = __uint_as_float(ps[1]);
#define PB(idx_) ((idx_) < NL ? dpp<NEWBC + ((idx_) % NL) * (16 / NL)>(pl) : dpp<NEWBC + ((idx_) % NL) * (16 / NL)>(phh))
#define PV_T(t_) do { float vf[8]; \
          _Pragma("unroll") for (int k = 0; k < 4; ++k) { vf[2 * k] = __builtin_bit_cast(float, V[s][t_][k] << 16); vf[2 * k + 1] = __builtin_bit_cast(float, V[s][t_][k] & 0xffff0000u); } \
          const float p0_ = PB(2 * (t_)), p1_ = PB(2 * (t_) + 1); \
          _Pragma("unroll") for (int k = 0; k < 8; ++k) { acc[k] = fmaf(p0_, vf[k], acc[k]); acc[8 + k] = fmaf(p1_, vf[k], acc[8 + k]); } } while (0)
        PV_T(0); PV_T(1); PV_T(2); PV_T(3);
        if constexpr (NL == 8) { PV_T(4 % NL); PV_T(5 % NL); PV_T(6 % NL); PV_T(7 % NL); }
#undef PV_T
#undef PB
        ISSUE(V, vc, s, nphys, nmeta & 255);
        meta[s] = nmeta;
        ++i;
      }
    };
    for (int gidx = cnt / DEPTH; gidx > 0; --gidx) {
      stage(IC<0>{});
      if constexpr (DEPTH > 1) stage(IC<1 % DEPTH>{});
      if constexpr (DEPTH > 2) stage(IC<2 % DEPTH>{});
      if constexpr (DEPTH > 3) stage(IC<3 % DEPTH>{});
    }
    if constexpr (DEPTH > 1) {
      if (i < cnt) {
        stage(IC<0>{});
        if constexpr (DEPTH > 2) {
          if (i < cnt) {
            stage(IC<1 % DEPTH>{});
            if constexpr (DEPTH > 3) { if (i < cnt) stage(IC<2 % DEPTH>{}); }
          }
        }
      }
    }
#undef ISSUE
    dump(cur, 1000);
  } else {
    const int lraw = lens[lane & 31];
    const u64 ep = __hip_atomic_load((GAS u64*)(ws + WS_EPOCH) + h * B + (lane & 31), RLX_AGENT);
    if (lane == 0) { lds_st(ctl + C_READY, 0); lds_st(ctl + C_SVC, 0); }
    if (lane == 0) { LAS float* rec = recs + (NREC - 1) * REC; rec[0] = -INFINITY; rec[1] = 0.f; rec[2] = -INFINITY; rec[3] = 0.f; }
    LGKM0();
    __builtin_amdgcn_s_barrier();
    const bool luni = lraw <= 0;
    const int llen = luni ? SKV : min(lraw, SKV);
    const int n = lane < 32 ? (llen + UNIT - 1) / UNIT : 0;
    const int P = __builtin_amdgcn_readlane(scan32(n), 31);
    const int T = max((P + B - 1) / B, R0);
    const int e = max(n - T, 0), sp = lane < 32 ? max(T - n, 0) : 0;
    const int sc = scan32(e | (sp << 16));
    const int Ein = sc & 0xffff, Sin = sc >> 16;
    const int E0 = Ein - e, S0 = Sin - sp;
    const int Etot = __builtin_amdgcn_readlane(Ein, 31);
    const int my_n = __builtin_amdgcn_readlane(n, b), my_len = __builtin_amdgcn_readlane(llen, b);
    const int hs = __builtin_amdgcn_readlane(S0, b), he = min(__builtin_amdgcn_readlane(Sin, b), Etot);
    const int nh = max(he - hs, 0);
    const int n_own = min(my_n, T);
    const int ntot = R0 + nh + max(0, n_own - R0);
    const int plo = max(E0, hs), phi = min(Ein, he);
    const u64 pmask = __ballot(lane < 32 && phi > plo);
    const int myE0 = __builtin_amdgcn_readlane(E0, b), myE1 = __builtin_amdgcn_readlane(Ein, b);
    const u64 hmask = __ballot(lane < 32 && min(min(Sin, Etot), myE1) > max(S0, myE0));
    constexpr int NP = NPOS / 64;
    int src[NP], un[NP], sg[NP], ln[NP];
#pragma unroll
    for (int k = 0; k < NP; ++k) {
      const int j = lane + 64 * k;
      src[k] = b; ln[k] = my_len;
      un[k] = j < R0 ? j : j - nh;
      sg[k] = (j < R0 || nh == 0) ? 0 : 255;
    }
    {
      u64 mk = pmask; int seg = 1;
      while (mk) {
        const int bp = __builtin_ctzll(mk); mk &= mk - 1;
        const int lo = __builtin_amdgcn_readlane(plo, bp), hi = __builtin_amdgcn_readlane(phi, bp);
        const int e0 = __builtin_amdgcn_readlane(E0, bp), bl = __builtin_amdgcn_readlane(llen, bp);
#pragma unroll
        for (int k = 0; k < NP; ++k) {
          const int xq = lane + 64 * k - R0 + hs;
          const bool in = xq >= lo && xq < hi;
          src[k] = in ? bp : src[k]; un[k] = in ? T + xq - e0 : un[k]; sg[k] = in ? seg : sg[k]; ln[k] = in ? bl : ln[k];
        }
        ++seg;
      }
    }
    int phys[NP];
#pragma unroll
    for (int k = 0; k < NP; ++k) phys[k] = bt[src[k] * NBPS + min(un[k] / UPP, NBPS - 1)];
    {
      u32x4 q0 = *reinterpret_cast<const u32x4*>(q + (size_t)(b * HQ + h * 2) * D + 8 * lane);
      u64 mk = pmask;
      u32x4 qq[3]; int bb[3];
#pragma unroll
      for (int k = 0; k < 3; ++k) {
        bb[k] = mk ? __builtin_ctzll(mk) : b; mk &= mk - 1;
        qq[k] = *reinterpret_cast<const u32x4*>(q + (size_t)(bb[k] * HQ + h * 2) * D + 8 * lane);
      }
      const u64 umask = __ballot(luni);
      const u32x4 z = {0u, 0u, 0u, 0u};
      *(LAS u32x4*)(sm + L_Q + lane * 16) = ((umask >> b) & 1) ? z : q0;
#pragma unroll
      for (int k = 0; k < 3; ++k) *(LAS u32x4*)(sm + L_Q + (k + 1) * 1024 + lane * 16) = ((umask >> bb[k]) & 1) ? z : qq[k];
      int seg = 4;
      while (mk) {
        const int bp = __builtin_ctzll(mk); mk &= mk - 1;
        const u32x4 qx = *reinterpret_cast<const u32x4*>(q + (size_t)(bp * HQ + h * 2) * D + 8 * lane);
        *(LAS u32x4*)(sm + L_Q + seg * 1024 + lane * 16) = ((umask >> bp) & 1) ? z : qx;
        ++seg;
      }
    }
#pragma unroll
    for (int k = 0; k < NP; ++k) {
      const int j = lane + 64 * k;
      const int rem = j < ntot ? max(0, min(UNIT, ln[k] - un[k] * UNIT)) : 0;
      *(LAS i32x2*)(sm + L_LIST + j * 8) = i32x2{phys[k] * UPP + (un[k] % UPP), rem | (sg[k] << 8)};
    }
    if (lane == 0) lds_st(ctl + C_NTOT, ntot);
    LGKM0();
    if (lane == 0) lds_st(ctl + C_READY, 1);

    const u64 tagv = ep * 6364136223846793005ull + 1442695040888963407ull;
    {
      u64 mk = pmask; int seg = 1;
      while (mk) {
        const int bp = __builtin_ctzll(mk); mk &= mk - 1;
        for (;;) {
          const int pr = lane < W ? lds_ld(ctl + C_PROG + lane) : 1000;
          if (__all(pr > seg)) break;
          __builtin_amdgcn_s_sleep(4);
        }
        asm volatile("" ::: "memory");
        LAS float* rb = recs + (2 * W + ((seg - 1) % NSLOT) * W) * REC;
        float M = -INFINITY, mw[W], lw[W];
#pragma unroll
        for (int k = 0; k < W; ++k) {
          const bool ok = ((LAS int*)(rb + k * REC))[4] == seg;
          mw[k] = ok ? rb[k * REC + 2 * r] : -INFINITY; lw[k] = ok ? rb[k * REC + 2 * r + 1] : 0.f;
          M = fmaxf(M, mw[k]);
        }
        float L = 0.f; f32x4 o0 = {0.f, 0.f, 0.f, 0.f}, o1 = o0;
#pragma unroll
        for (int k = 0; k < W; ++k) {
          const bool ok = lw[k] > 0.f;
          const float wt = ok ? __builtin_amdgcn_exp2f(mw[k] - M) : 0.f;
          f32x4 a0 = *(LAS f32x4*)(rb + k * REC + 8 + lane * 8), a1 = *(LAS f32x4*)(rb + k * REC + 8 + lane * 8 + 4);
          const f32x4 z = {0.f, 0.f, 0.f, 0.f};
          a0 = ok ? a0 : z; a1 = ok ? a1 : z;
          L = fmaf(wt, lw[k], L); o0 += wt * a0; o1 += wt * a1;
        }
        LGKM0();
        if (lane == 0) lds_st(ctl + C_SVC, seg);
        const int slot = (h * B + bp) * B + b;
        const __amdgpu_buffer_rsrc_t rs = __builtin_amdgcn_make_buffer_rsrc((void*)(ws + WS_PAY + (size_t)slot * PAY_STRIDE), 0, PAY_STRIDE, 0x00020000);
        __builtin_amdgcn_raw_buffer_store_b128(__builtin_bit_cast(u32x4, o0), rs, 64 + lane * 32, 0, 16);
        __builtin_amdgcn_raw_buffer_store_b128(__builtin_bit_cast(u32x4, o1), rs, 64 + lane * 32 + 16, 0, 16);
        if (c == 0) __builtin_amdgcn_raw_buffer_store_b64(__builtin_bit_cast(__attribute__((ext_vector_type(2))) unsigned, f32x2{M, L}), rs, r * 8, 0, 16);
        VM0();
        const unsigned tlo = __builtin_amdgcn_readlane((unsigned)tagv, bp), thi = __builtin_amdgcn_readlane((unsigned)(tagv >> 32), bp);
        if (lane == 0) __hip_atomic_store((GAS u64*)(ws + WS_FLAG) + slot, ((u64)thi << 32) | tlo, RLX_AGENT);
        ++seg;
      }
    }
    if (hmask) {
      const unsigned tlo = __builtin_amdgcn_readlane((unsigned)tagv, b), thi = __builtin_amdgcn_readlane((unsigned)(tagv >> 32), b);
      const u64 tag = ((u64)thi << 32) | tlo;
      const bool mine = (hmask >> lane) & 1;
      for (unsigned spins = 0; spins < (1u << 22); ++spins) {
        const u64 f = __hip_atomic_load((GAS u64*)(ws + WS_FLAG) + (h * B + b) * B + (lane & 31), RLX_AGENT);
        if (__all(!mine || f == tag)) break;
        __builtin_amdgcn_s_sleep(8);
      }
      __builtin_amdgcn_fence(__ATOMIC_ACQUIRE, "agent");
      float M = -INFINITY, L = 0.f; f32x4 o0 = {0.f, 0.f, 0.f, 0.f}, o1 = o0;
      u64 mk = hmask;
      while (mk) {
        const int hb = __builtin_ctzll(mk); mk &= mk - 1;
        const int slot = (h * B + b) * B + hb;
        const __amdgpu_buffer_rsrc_t rs = __builtin_amdgcn_make_buffer_rsrc((void*)(ws + WS_PAY + (size_t)slot * PAY_STRIDE), 0, PAY_STRIDE, 0x00020000);
        const f32x4 a0 = __builtin_bit_cast(f32x4, __builtin_amdgcn_raw_buffer_load_b128(rs, 64 + lane * 32, 0, 16));
        const f32x4 a1 = __builtin_bit_cast(f32x4, __builtin_amdgcn_raw_buffer_load_b128(rs, 64 + lane * 32 + 16, 0, 16));
        const f32x2 ml = __builtin_bit_cast(f32x2, __builtin_amdgcn_raw_buffer_load_b64(rs, r * 8, 0, 16));
        const float Mn = fmaxf(M, ml.x);
        const float wo = (L > 0.f) ? __builtin_amdgcn_exp2f(M - Mn) : 0.f, wn = (ml.y > 0.f) ? __builtin_amdgcn_exp2f(ml.x - Mn) : 0.f;
        L = L * wo + ml.y * wn; o0 = o0 * wo + a0 * wn; o1 = o1 * wo + a1 * wn; M = Mn;
      }
      LAS float* rec = recs + (NREC - 1) * REC;
      *(LAS f32x4*)(rec + 8 + lane * 8) = o0;
      *(LAS f32x4*)(rec + 8 + lane * 8 + 4) = o1;
      if (c == 0) *(LAS f32x2*)(rec + 2 * r) = f32x2{M, L};
      if (lane == 0) __hip_atomic_store((GAS u64*)(ws + WS_EPOCH) + h * B + b, tag, RLX_AGENT);
    }
  }
  LGKM0();
  __builtin_amdgcn_s_barrier();
  asm volatile("" ::: "memory");
  if (tid < 256) {
    const int g = tid >> 7, d0 = 2 * (tid & 127);
    constexpr int NF = 2 * W + 1;
    float mw[NF], lw[NF], M = -INFINITY;
#pragma unroll
    for (int k = 0; k < NF; ++k) {
      const int ri = k < 2 * W ? k : NREC - 1;
      const f32x2 ml = *(LAS f32x2*)(recs + ri * REC + 2 * g);
      mw[k] = ml.x; lw[k] = ml.y; M = fmaxf(M, ml.x);
    }
    float L = 0.f, o0 = 0.f, o1 = 0.f;
#pragma unroll
    for (int k = 0; k < NF; ++k) {
      const int ri = k < 2 * W ? k : NREC - 1;
      const bool ok = lw[k] > 0.f;
      const float wt = ok ? __builtin_amdgcn_exp2f(mw[k] - M) : 0.f;
      const f32x2 a = *(LAS f32x2*)(recs + ri * REC + 8 + g * D + d0);
      L = fmaf(wt, lw[k], L); o0 = fmaf(wt, ok ? a.x : 0.f, o0); o1 = fmaf(wt, ok ? a.y : 0.f, o1);
    }
    const float inv = 1.f / L;
    __hip_bfloat162 o; o.x = __float2bfloat16(o0 * inv); o.y = __float2bfloat16(o1 * inv);
    *reinterpret_cast<__hip_bfloat162*>(out + (size_t)(b * HQ + h * 2 + g) * D + d0) = o;
  }
}

extern "C" void kernel_launch(void* const* d_in, const int* in_sizes, int n_in, void* d_out, int out_size, void* d_ws,
                              size_t ws_size, hipStream_t stream) {
  attn_kernel<<<B * HKV, NTHR, 0, stream>>>((const uint16_t*)d_in[0], (const char*)d_in[1], (const char*)d_in[2],
                                            (const int*)d_in[3], (const int*)d_in[4], (uint16_t*)d_out, (char*)d_ws);
}
```

```cpp
#include <hip/hip_runtime.h>
#include <hip/hip_bf16.h>
#include <cstdint>

#define CFG_W 4
#define CFG_UNIT 16
#define CFG_DEPTH 2

constexpr int B = 32, HQ = 16, HKV = 8, D = 256, PAGE = 16, NBPS = 128, SKV = NBPS * PAGE;
constexpr int W = CFG_W, UNIT = CFG_UNIT, DEPTH = CFG_DEPTH;
constexpr int UPP = PAGE / UNIT;
constexpr int NL = UNIT / 2;
constexpr int NU = SKV / UNIT;
constexpr int R0 = W * DEPTH;
constexpr int NPOS = ((NU + 2 * R0 + 63) / 64) * 64;
constexpr int NSLOT = 2;
constexpr int AUX_K = 2, AUX_V = 0;
constexpr int REC = 8 + 2 * D;
constexpr int NREC = 2 * W + NSLOT * W + 1;
constexpr int NTHR = (W + 1) * 64;

constexpr int L_CTL = 0;
constexpr int L_LIST = 256;
constexpr int L_Q = L_LIST + NPOS * 8;
constexpr int L_BT = L_Q + 33 * 1024;
constexpr int L_REC = L_BT + B * NBPS * 4;
constexpr int L_END = L_REC + NREC * REC * 4;
constexpr int C_READY = 0, C_NTOT = 1, C_SVC = 2, C_QRDY = 3, C_PROG = 8;

constexpr size_t WS_EPOCH = 0;
constexpr size_t WS_FLAG = 4096;
constexpr size_t WS_PAY = 4096 + 65536;
constexpr int PAY_STRIDE = 2176;

using bf16x2 = __attribute__((ext_vector_type(2))) __bf16;
using u32x4 = __attribute__((ext_vector_type(4))) unsigned;
using f32x4 = __attribute__((ext_vector_type(4))) float;
using f32x2 = __attribute__((ext_vector_type(2))) float;
using i32x2 = __attribute__((ext_vector_type(2))) int;
typedef unsigned long long u64;
template <int N> struct IC { static constexpr int value = N; };
#define GAS __attribute__((address_space(1)))
#define LAS __attribute__((address_space(3)))
#define RLX_AGENT __ATOMIC_RELAXED, __HIP_MEMORY_SCOPE_AGENT
#define RLX_WG __ATOMIC_RELAXED, __HIP_MEMORY_SCOPE_WORKGROUP

__device__ __forceinline__ float dot2(unsigned k, unsigned q, float acc) {
  return __builtin_amdgcn_fdot2_f32_bf16(__builtin_bit_cast(bf16x2, k), __builtin_bit_cast(bf16x2, q), acc, false);
}
template <int CTRL> __device__ __forceinline__ float dpp(float x) {
  return __builtin_bit_cast(float, __builtin_amdgcn_mov_dpp(__builtin_bit_cast(int, x), CTRL, 0xf, 0xf, true));
}
template <int CTRL> __device__ __forceinline__ int dppi(int x) {
  return __builtin_amdgcn_update_dpp(0, x, CTRL, 0xf, 0xf, true);
}
constexpr int XOR1 = 0xB1, XOR2 = 0x4E, XOR7 = 0x141, XOR8 = 0x128, ROR4 = 0x124, NEWBC = 0x150;

__device__ __forceinline__ float sum16swap(float a, float b) {
  auto s = __builtin_amdgcn_permlane16_swap(__float_as_uint(a), __float_as_uint(b), false, false);
  return __uint_as_float(s[0]) + __uint_as_float(s[1]);
}
__device__ __forceinline__ float sum32swap(float a, float b) {
  auto s = __builtin_amdgcn_permlane32_swap(__float_as_uint(a), __float_as_uint(b), false, false);
  return __uint_as_float(s[0]) + __uint_as_float(s[1]);
}
__device__ __forceinline__ float max16(float a) {
  auto s = __builtin_amdgcn_permlane16_swap(__float_as_uint(a), __float_as_uint(a), false, false);
  return fmaxf(__uint_as_float(s[0]), __uint_as_float(s[1]));
}
__device__ __forceinline__ float max32(float a) {
  auto s = __builtin_amdgcn_permlane32_swap(__float_as_uint(a), __float_as_uint(a), false, false);
  return fmaxf(__uint_as_float(s[0]), __uint_as_float(s[1]));
}
__device__ __forceinline__ int lds_ld(LAS int* p) { return __hip_atomic_load(p, RLX_WG); }
__device__ __forceinline__ void lds_st(LAS int* p, int v) { __hip_atomic_store(p, v, RLX_WG); }
#define LGKM0() asm volatile("s_waitcnt lgkmcnt(0)" ::: "memory")
#define VM0() asm volatile("s_waitcnt vmcnt(0)" ::: "memory")
#define ST(k) do {} while (0)

__device__ __forceinline__ int scan32(int x) {
  x += dppi<0x111>(x); x += dppi<0x112>(x); x += dppi<0x114>(x); x += dppi<0x118>(x);
  const int r0 = __builtin_amdgcn_readlane(x, 15);
  return x + (((threadIdx.x & 63) >= 16) ? r0 : 0);
}

__global__ void __launch_bounds__(NTHR, (W + 4) / 4) attn_kernel(const uint16_t* __restrict__ q, const char* __restrict__ kc,
                                                    const char* __restrict__ vc, const int* __restrict__ bt,
                                                    const int* __restrict__ lens, uint16_t* __restrict__ out,
                                                    char* __restrict__ ws) {
  __shared__ __attribute__((aligned(16))) char smem[L_END];
  LAS char* sm = (LAS char*)smem;
  LAS int* ctl = (LAS int*)(sm + L_CTL);
  LAS float* recs = (LAS float*)(sm + L_REC);
  const int b = blockIdx.x >> 3, h = blockIdx.x & 7;
  const int tid = threadIdx.x, lane = tid & 63, wave = __builtin_amdgcn_readfirstlane(tid >> 6);
  const int r = lane >> 5, c = lane & 31;
  constexpr float C2 = 0.0625f * 1.4426950408889634f;
  ST(0);

  if (wave < W) {
    const int w = wave;
    const int len_raw = lens[b];
    int ph0[DEPTH];
#pragma unroll
    for (int k = 0; k < DEPTH; ++k) { const int u = w + W * k; ph0[k] = bt[b * NBPS + u / UPP] * UPP + (u % UPP); }
    u32x4 qv[2];
#pragma unroll
    for (int g = 0; g < 2; ++g) qv[g] = *reinterpret_cast<const u32x4*>(q + (size_t)(b * HQ + h * 2 + g) * D + 8 * c);
    if (lane < 2 + NSLOT) {
      const int ri = lane < 2 ? lane * W + w : 2 * W + (lane - 2) * W + w;
      LAS float* rec = recs + ri * REC;
      rec[0] = -INFINITY; rec[1] = 0.f; rec[2] = -INFINITY; rec[3] = 0.f; ((LAS int*)rec)[4] = -1;
    }
    if (lane == 0) lds_st(ctl + C_PROG + w, 0);
    LGKM0();
    __builtin_amdgcn_s_barrier();
    const bool uni = len_raw <= 0;
    const int len = uni ? SKV : min(len_raw, SKV);
    const int voff = r * 4096 + h * 512 + c * 16;
    u32x4 K[DEPTH][NL], V[DEPTH][NL];
    int meta[DEPTH];
#define ISSUE(BUF, base_, s_, phys_, rem_) do { constexpr int AUX_ = AUX_##BUF; \
      const __amdgpu_buffer_rsrc_t rs_ = __builtin_amdgcn_make_buffer_rsrc((void*)((base_) + (size_t)(unsigned)(phys_) * (UNIT * 4096)), 0, (rem_) * 4096, 0x00020000); \
      _Pragma("unroll") for (int t_ = 0; t_ < NL; ++t_) BUF[s_][t_] = __builtin_amdgcn_raw_buffer_load_b128(rs_, voff, t_ * 8192, AUX_); } while (0)
#pragma unroll
    for (int k = 0; k < DEPTH; ++k) {
      const int u = w + W * k;
      const int rem = __builtin_amdgcn_readfirstlane(max(0, min(UNIT, len - u * UNIT)));
      meta[k] = rem;
      ISSUE(K, kc, k, ph0[k], rem);
      ISSUE(V, vc, k, ph0[k], rem);
    }
    ST(1);
    if (uni) { qv[0] = u32x4{0u, 0u, 0u, 0u}; qv[1] = qv[0]; }

    float m = -INFINITY, l = 0.f, acc[16];
#pragma unroll
    for (int k = 0; k < 16; ++k) acc[k] = 0.f;
    int cur = 0;
    while (lds_ld(ctl + C_READY) == 0) __builtin_amdgcn_s_sleep(1);
    asm volatile("" ::: "memory");
    ST(2);
    const int ntot = __builtin_amdgcn_readfirstlane(lds_ld(ctl + C_NTOT));
    const int cnt = (ntot - w + W - 1) / W;

    auto dump = [&](int seg, int next) __attribute__((always_inline)) {
      int ri;
      if (seg == 0) ri = w;
      else if (seg == 255) ri = W + w;
      else {
        ri = 2 * W + ((seg - 1) % NSLOT) * W + w;
        while (lds_ld(ctl + C_SVC) < seg - NSLOT) __builtin_amdgcn_s_sleep(1);
        asm volatile("" ::: "memory");
      }
      LAS float* rec = recs + ri * REC;
      float o[8];
#pragma unroll
      for (int k = 0; k < 8; ++k) o[k] = sum32swap(acc[k], acc[8 + k]);
      float t = l;
      if constexpr (UNIT == 16) t += dpp<ROR4>(t);
      t += dpp<XOR8>(t); t = sum16swap(t, t); t = sum32swap(t, t);
      *(LAS f32x4*)(rec + 8 + lane * 8) = f32x4{o[0], o[1], o[2], o[3]};
      *(LAS f32x4*)(rec + 8 + lane * 8 + 4) = f32x4{o[4], o[5], o[6], o[7]};
      constexpr int G1 = UNIT == 16 ? 2 : 4;
      if (lane == 0 || lane == G1) *(LAS f32x2*)(rec + (lane ? 2 : 0)) = f32x2{m, t};
      if (lane == 0) ((LAS int*)rec)[4] = seg;
      LGKM0();
      if (lane == 0) lds_st(ctl + C_PROG + w, next);
    };

    int i = 0;
    auto stage = [&](auto sc_) __attribute__((always_inline)) {
      {
        constexpr int s = decltype(sc_)::value;
        const i32x2 ent = *(LAS i32x2*)(sm + L_LIST + (w + W * (i + DEPTH)) * 8);
        const int rem = meta[s] & 255, seg = meta[s] >> 8;
        if (seg != cur) {
          dump(cur, seg);
          m = -INFINITY; l = 0.f;
#pragma unroll
          for (int k = 0; k < 16; ++k) acc[k] = 0.f;
          const int qi = seg == 255 ? 0 : seg;
          while (lds_ld(ctl + C_QRDY) == 0) __builtin_amdgcn_s_sleep(1);
          asm volatile("" ::: "memory");
          qv[0] = *(LAS u32x4*)(sm + L_Q + qi * 1024 + c * 16);
          qv[1] = *(LAS u32x4*)(sm + L_Q + qi * 1024 + 512 + c * 16);
          cur = seg;
        }
        float x[2 * NL];
#pragma unroll
        for (int t = 0; t < NL; ++t)
#pragma unroll
          for (int g = 0; g < 2; ++g) {
            float d = 0.f;
#pragma unroll
            for (int k = 0; k < 4; ++k) d = dot2(K[s][t][k], qv[g][k], d);
            x[t * 2 + g] = d;
          }
        const int nphys = __builtin_amdgcn_readfirstlane(ent.x), nmeta = __builtin_amdgcn_readfirstlane(ent.y);
        ISSUE(K, kc, s, nphys, nmeta & 255);
#pragma unroll
        for (int hh = 0; hh < NL; ++hh) x[hh] = sum16swap(x[hh], x[hh + NL]);
#define TR_STEP(HALF, CTRL, BIT) _Pragma("unroll") for (int hh = 0; hh < (HALF); ++hh) { \
          const float lo_ = x[hh], hi_ = x[hh + (HALF)]; const float t1_ = lo_ + dpp<CTRL>(lo_), t2_ = hi_ + dpp<CTRL>(hi_); \
          x[hh] = (c & (BIT)) ? t2_ : t1_; }
        float sc; int tok;
        if constexpr (UNIT == 16) {
          TR_STEP(4, XOR8, 8) TR_STEP(2, XOR7, 4) TR_STEP(1, XOR2, 2)
          sc = x[0] + dpp<XOR1>(x[0]);
          tok = 2 * (c >> 2) + r;
        } else {
          TR_STEP(2, XOR8, 8) TR_STEP(1, XOR7, 4)
          sc = x[0] + dpp<XOR2>(x[0]);
          sc = sc + dpp<XOR1>(sc);
          tok = 2 * (c >> 3) + r;
        }
#undef TR_STEP
        sc = (tok < rem) ? sc * C2 : -INFINITY;
        float pm = sc;
        if constexpr (UNIT == 16) pm = fmaxf(pm, dpp<ROR4>(pm));
        pm = fmaxf(pm, dpp<XOR8>(pm)); pm = max16(pm); pm = max32(pm);
        const float mn = fmaxf(m, pm), msub = (mn == -INFINITY) ? 0.f : mn;
        const float alpha = __builtin_amdgcn_exp2f(m - msub), p = __builtin_amdgcn_exp2f(sc - msub);
        l = fmaf(l, alpha, p); m = mn;
        const float a0 = __builtin_bit_cast(float, __builtin_amdgcn_readlane(__builtin_bit_cast(int, alpha), 0));
        const float a1 = __builtin_bit_cast(float, __builtin_amdgcn_readlane(__builtin_bit_cast(int, alpha), UNIT == 16 ? 2 : 4));
        if (__builtin_bit_cast(int, a0) != 0x3f800000 || __builtin_bit_cast(int, a1) != 0x3f800000) {
#pragma unroll
          for (int k = 0; k < 8; ++k) { acc[k] *= a0; acc[8 + k] *= a1; }
        }
        auto ps = __builtin_amdgcn_permlane16_swap(__float_as_uint(p), __float_as_uint(p), false, false);
        const float pl = __uint_as_float(ps[0]), phh = __uint_as_float(ps[1]);
#define PB(idx_) ((idx_) < NL ? dpp<NEWBC + ((idx_) % NL) * (16 / NL)>(pl) : dpp<NEWBC + ((idx_) % NL) * (16 / NL)>(phh))
#define PV_T(t_) do { float vf[8]; \
          _Pragma("unroll") for (int k = 0; k < 4; ++k) { vf[2 * k] = __builtin_bit_cast(float, V[s][t_][k] << 16); vf[2 * k + 1] = __builtin_bit_cast(float, V[s][t_][k] & 0xffff0000u); } \
          const float p0_ = PB(2 * (t_)), p1_ = PB(2 * (t_) + 1); \
          _Pragma("unroll") for (int k = 0; k < 8; ++k) { acc[k] = fmaf(p0_, vf[k], acc[k]); acc[8 + k] = fmaf(p1_, vf[k], acc[8 + k]); } } while (0)
        PV_T(0); PV_T(1); PV_T(2); PV_T(3);
        if constexpr (NL == 8) { PV_T(4 % NL); PV_T(5 % NL); PV_T(6 % NL); PV_T(7 % NL); }
#undef PV_T
#undef PB
        ISSUE(V, vc, s, nphys, nmeta & 255);
        meta[s] = nmeta;
        ++i;
      }
    };
    for (int gidx = cnt / DEPTH; gidx > 0; --gidx) {
      stage(IC<0>{});
      if constexpr (DEPTH > 1) stage(IC<1 % DEPTH>{});
      if constexpr (DEPTH > 2) stage(IC<2 % DEPTH>{});
      if constexpr (DEPTH > 3) stage(IC<3 % DEPTH>{});
    }
    if constexpr (DEPTH > 1) {
      if (i < cnt) {
        stage(IC<0>{});
        if constexpr (DEPTH > 2) {
          if (i < cnt) {
            stage(IC<1 % DEPTH>{});
            if constexpr (DEPTH > 3) { if (i < cnt) stage(IC<2 % DEPTH>{}); }
          }
        }
      }
    }
#undef ISSUE
    ST(4);
    dump(cur, 1000);
  } else {
    const int lraw = lens[lane & 31];
    u32x4 btr[16];
#pragma unroll
    for (int k = 0; k < 16; ++k) btr[k] = *reinterpret_cast<const u32x4*>(bt + k * 256 + lane * 4);
    const u64 ep = __hip_atomic_load((GAS u64*)(ws + WS_EPOCH) + h * B + (lane & 31), RLX_AGENT);
    if (lane == 0) { lds_st(ctl + C_READY, 0); lds_st(ctl + C_SVC, 0); lds_st(ctl + C_QRDY, 0); }
    if (lane == 0) { LAS float* rec = recs + (NREC - 1) * REC; rec[0] = -INFINITY; rec[1] = 0.f; rec[2] = -INFINITY; rec[3] = 0.f; }
    LGKM0();
    __builtin_amdgcn_s_barrier();
    ST(1);
    const bool luni = lraw <= 0;
    const int llen = luni ? SKV : min(lraw, SKV);
    const int n = lane < 32 ? (llen + UNIT - 1) / UNIT : 0;
    const int P = __builtin_amdgcn_readlane(scan32(n), 31);
    const int T = max((P + B - 1) / B, R0);
    const int e = max(n - T, 0), sp = lane < 32 ? max(T - n, 0) : 0;
    const int sc = scan32(e | (sp << 16));
    const int Ein = sc & 0xffff, Sin = sc >> 16;
    const int E0 = Ein - e, S0 = Sin - sp;
    const int Etot = __builtin_amdgcn_readlane(Ein, 31);
    const int my_n = __builtin_amdgcn_readlane(n, b), my_len = __builtin_amdgcn_readlane(llen, b);
    const int hs = __builtin_amdgcn_readlane(S0, b), he = min(__builtin_amdgcn_readlane(Sin, b), Etot);
    const int nh = max(he - hs, 0);
    const int n_own = min(my_n, T);
    const int ntot = R0 + nh + max(0, n_own - R0);
    const int plo = max(E0, hs), phi = min(Ein, he);
    const u64 pmask = __ballot(lane < 32 && phi > plo);
    const int myE0 = __builtin_amdgcn_readlane(E0, b), myE1 = __builtin_amdgcn_readlane(Ein, b);
    const u64 hmask = __ballot(lane < 32 && min(min(Sin, Etot), myE1) > max(S0, myE0));
    const u64 omask = __ballot(e > 0), smask = __ballot(sp > 0);
    constexpr int NP = NPOS / 64;
    int src[NP], un[NP], sg[NP], ln[NP];
#pragma unroll
    for (int k = 0; k < NP; ++k) {
      const int j = lane + 64 * k;
      src[k] = b; ln[k] = my_len;
      un[k] = j < R0 ? j : j - nh;
      sg[k] = (j < R0 || nh == 0) ? 0 : 255;
    }
    {
      u64 mk = pmask; int seg = 1;
      while (mk) {
        const int bp = __builtin_ctzll(mk); mk &= mk - 1;
        const int lo = __builtin_amdgcn_readlane(plo, bp), hi = __builtin_amdgcn_readlane(phi, bp);
        const int e0 = __builtin_amdgcn_readlane(E0, bp), bl = __builtin_amdgcn_readlane(llen, bp);
#pragma unroll
        for (int k = 0; k < NP; ++k) {
          const int xq = lane + 64 * k - R0 + hs;
          const bool in = xq >= lo && xq < hi;
          src[k] = in ? bp : src[k]; un[k] = in ? T + xq - e0 : un[k]; sg[k] = in ? seg : sg[k]; ln[k] = in ? bl : ln[k];
        }
        ++seg;
      }
    }
    ST(2);
    int phys[NP];
#pragma unroll
    for (int k = 0; k < 16; ++k) *(LAS u32x4*)(sm + L_BT + k * 1024 + lane * 16) = btr[k];
    LGKM0();
#pragma unroll
    for (int k = 0; k < NP; ++k) phys[k] = *(LAS int*)(sm + L_BT + (src[k] * NBPS + min(un[k] / UPP, NBPS - 1)) * 4);
#pragma unroll
    for (int k = 0; k < NP; ++k) {
      const int j = lane + 64 * k;
      const int rem = j < ntot ? max(0, min(UNIT, ln[k] - un[k] * UNIT)) : 0;
      *(LAS i32x2*)(sm + L_LIST + j * 8) = i32x2{phys[k] * UPP + (un[k] % UPP), rem | (sg[k] << 8)};
    }
    if (lane == 0) lds_st(ctl + C_NTOT, ntot);
    LGKM0();
    if (lane == 0) lds_st(ctl + C_READY, 1);
    ST(3);
    if (pmask) {
    {
      u32x4 q0 = *reinterpret_cast<const u32x4*>(q + (size_t)(b * HQ + h * 2) * D + 8 * lane);
      u64 mk = pmask;
      u32x4 qq[3]; int bb[3];
#pragma unroll
      for (int k = 0; k < 3; ++k) {
        bb[k] = mk ? __builtin_ctzll(mk) : b; mk &= mk - 1;
        qq[k] = *reinterpret_cast<const u32x4*>(q + (size_t)(bb[k] * HQ + h * 2) * D + 8 * lane);
      }
      const u64 umask = __ballot(luni);
      const u32x4 z = {0u, 0u, 0u, 0u};
      *(LAS u32x4*)(sm + L_Q + lane * 16) = ((umask >> b) & 1) ? z : q0;
#pragma unroll
      for (int k = 0; k < 3; ++k) *(LAS u32x4*)(sm + L_Q + (k + 1) * 1024 + lane * 16) = ((umask >> bb[k]) & 1) ? z : qq[k];
      int seg = 4;
      while (mk) {
        const int bp = __builtin_ctzll(mk); mk &= mk - 1;
        const u32x4 qx = *reinterpret_cast<const u32x4*>(q + (size_t)(bp * HQ + h * 2) * D + 8 * lane);
        *(LAS u32x4*)(sm + L_Q + seg * 1024 + lane * 16) = ((umask >> bp) & 1) ? z : qx;
        ++seg;
      }
    }
      LGKM0();
      if (lane == 0) lds_st(ctl + C_QRDY, 1);
    }
    ST(4);

    const u64 tagv = ep * 6364136223846793005ull + 1442695040888963407ull;
    {
      u64 mk = pmask; int seg = 1;
      while (mk) {
        const int bp = __builtin_ctzll(mk); mk &= mk - 1;
        for (;;) {
          const int pr = lane < W ? lds_ld(ctl + C_PROG + lane) : 1000;
          if (__all(pr > seg)) break;
          __builtin_amdgcn_s_sleep(4);
        }
        asm volatile("" ::: "memory");
        LAS float* rb = recs + (2 * W + ((seg - 1) % NSLOT) * W) * REC;
        float M = -INFINITY, mw[W], lw[W];
#pragma unroll
        for (int k = 0; k < W; ++k) {
          const bool ok = ((LAS int*)(rb + k * REC))[4] == seg;
          mw[k] = ok ? rb[k * REC + 2 * r] : -INFINITY; lw[k] = ok ? rb[k * REC + 2 * r + 1] : 0.f;
          M = fmaxf(M, mw[k]);
        }
        float L = 0.f; f32x4 o0 = {0.f, 0.f, 0.f, 0.f}, o1 = o0;
#pragma unroll
        for (int k = 0; k < W; ++k) {
          const bool ok = lw[k] > 0.f;
          const float wt = ok ? __builtin_amdgcn_exp2f(mw[k] - M) : 0.f;
          f32x4 a0 = *(LAS f32x4*)(rb + k * REC + 8 + lane * 8), a1 = *(LAS f32x4*)(rb + k * REC + 8 + lane * 8 + 4);
          const f32x4 z = {0.f, 0.f, 0.f, 0.f};
          a0 = ok ? a0 : z; a1 = ok ? a1 : z;
          L = fmaf(wt, lw[k], L); o0 += wt * a0; o1 += wt * a1;
        }
        LGKM0();
        if (lane == 0) lds_st(ctl + C_SVC, seg);
        const int slot = (h * B + bp) * B + b;
        const int pid = h * 64 + __builtin_popcountll(omask & ((1ull << bp) - 1)) + __builtin_popcountll(smask & ((1ull << b) - 1));
        const __amdgpu_buffer_rsrc_t rs = __builtin_amdgcn_make_buffer_rsrc((void*)(ws + WS_PAY + (size_t)pid * PAY_STRIDE), 0, PAY_STRIDE, 0x00020000);
        __builtin_amdgcn_raw_buffer_store_b128(__builtin_bit_cast(u32x4, o0), rs, 128 + lane * 32, 0, 16);
        __builtin_amdgcn_raw_buffer_store_b128(__builtin_bit_cast(u32x4, o1), rs, 128 + lane * 32 + 16, 0, 16);
        if (c == 0) __builtin_amdgcn_raw_buffer_store_b64(__builtin_bit_cast(__attribute__((ext_vector_type(2))) unsigned, f32x2{M, L}), rs, r * 8, 0, 16);
        VM0();
        const unsigned tlo = __builtin_amdgcn_readlane((unsigned)tagv, bp), thi = __builtin_amdgcn_readlane((unsigned)(tagv >> 32), bp);
        if (lane == 0) __hip_atomic_store((GAS u64*)(ws + WS_FLAG) + slot, ((u64)thi << 32) | tlo, RLX_AGENT);
        ++seg;
      }
    }
    if (hmask) {
      const unsigned tlo = __builtin_amdgcn_readlane((unsigned)tagv, b), thi = __builtin_amdgcn_readlane((unsigned)(tagv >> 32), b);
      const u64 tag = ((u64)thi << 32) | tlo;
      const bool mine = (hmask >> lane) & 1;
      for (unsigned spins = 0; spins < (1u << 22); ++spins) {
        const u64 f = __hip_atomic_load((GAS u64*)(ws + WS_FLAG) + (h * B + b) * B + (lane & 31), RLX_AGENT);
        if (__all(!mine || f == tag)) break;
        __builtin_amdgcn_s_sleep(8);
      }
      __builtin_amdgcn_fence(__ATOMIC_ACQUIRE, "agent");
      float M = -INFINITY, L = 0.f; f32x4 o0 = {0.f, 0.f, 0.f, 0.f}, o1 = o0;
      u64 mk = hmask;
      while (mk) {
        const int hb = __builtin_ctzll(mk); mk &= mk - 1;
        const int pid = h * 64 + __builtin_popcountll(omask & ((1ull << b) - 1)) + __builtin_popcountll(smask & ((1ull << hb) - 1));
        const __amdgpu_buffer_rsrc_t rs = __builtin_amdgcn_make_buffer_rsrc((void*)(ws + WS_PAY + (size_t)pid * PAY_STRIDE), 0, PAY_STRIDE, 0x00020000);
        const f32x4 a0 = __builtin_bit_cast(f32x4, __builtin_amdgcn_raw_buffer_load_b128(rs, 128 + lane * 32, 0, 16));
        const f32x4 a1 = __builtin_bit_cast(f32x4, __builtin_amdgcn_raw_buffer_load_b128(rs, 128 + lane * 32 + 16, 0, 16));
        const f32x2 ml = __builtin_bit_cast(f32x2, __builtin_amdgcn_raw_buffer_load_b64(rs, r * 8, 0, 16));
        const float Mn = fmaxf(M, ml.x);
        const float wo = (L > 0.f) ? __builtin_amdgcn_exp2f(M - Mn) : 0.f, wn = (ml.y > 0.f) ? __builtin_amdgcn_exp2f(ml.x - Mn) : 0.f;
        L = L * wo + ml.y * wn; o0 = o0 * wo + a0 * wn; o1 = o1 * wo + a1 * wn; M = Mn;
      }
      LAS float* rec = recs + (NREC - 1) * REC;
      *(LAS f32x4*)(rec + 8 + lane * 8) = o0;
      *(LAS f32x4*)(rec + 8 + lane * 8 + 4) = o1;
      if (c == 0) *(LAS f32x2*)(rec + 2 * r) = f32x2{M, L};
      if (lane == 0) __hip_atomic_store((GAS u64*)(ws + WS_EPOCH) + h * B + b, tag, RLX_AGENT);
    }
  }
  ST(5);
  LGKM0();
  __builtin_amdgcn_s_barrier();
  asm volatile("" ::: "memory");
  ST(6);
  if (tid < 256) {
    const int g = tid >> 7, d0 = 2 * (tid & 127);
    constexpr int NF = 2 * W + 1;
    float mw[NF], lw[NF], M = -INFINITY;
#pragma unroll
    for (int k = 0; k < NF; ++k) {
      const int ri = k < 2 * W ? k : NREC - 1;
      const f32x2 ml = *(LAS f32x2*)(recs + ri * REC + 2 * g);
      mw[k] = ml.x; lw[k] = ml.y; M = fmaxf(M, ml.x);
    }
    float L = 0.f, o0 = 0.f, o1 = 0.f;
#pragma unroll
    for (int k = 0; k < NF; ++k) {
      const int ri = k < 2 * W ? k : NREC - 1;
      const bool ok = lw[k] > 0.f;
      const float wt = ok ? __builtin_amdgcn_exp2f(mw[k] - M) : 0.f;
      const f32x2 a = *(LAS f32x2*)(recs + ri * REC + 8 + g * D + d0);
      L = fmaf(wt, lw[k], L); o0 = fmaf(wt, ok ? a.x : 0.f, o0); o1 = fmaf(wt, ok ? a.y : 0.f, o1);
    }
    const float inv = 1.f / L;
    __hip_bfloat162 o; o.x = __float2bfloat16(o0 * inv); o.y = __float2bfloat16(o1 * inv);
    *reinterpret_cast<__hip_bfloat162*>(out + (size_t)(b * HQ + h * 2 + g) * D + d0) = o;
  }
}

extern "C" void kernel_launch(void* const* d_in, const int* in_sizes, int n_in, void* d_out, int out_size, void* d_ws,
                              size_t ws_size, hipStream_t stream) {
  attn_kernel<<<B * HKV, NTHR, 0, stream>>>((const uint16_t*)d_in[0], (const char*)d_in[1], (const char*)d_in[2],
                                            (const int*)d_in[3], (const int*)d_in[4], (uint16_t*)d_out, (char*)d_ws);
}
```

```cpp
#include <hip/hip_runtime.h>
#include <hip/hip_bf16.h>
#include <cstdint>

#define CFG_W 4
#define CFG_UNIT 16
#define CFG_DEPTH 2

constexpr int B = 32, HQ = 16, HKV = 8, D = 256, PAGE = 16, NBPS = 128, SKV = NBPS * PAGE;
constexpr int W = CFG_W, UNIT = CFG_UNIT, DEPTH = CFG_DEPTH;
constexpr int UPP = PAGE / UNIT;
constexpr int NL = UNIT / 2;
constexpr int NU = SKV / UNIT;
constexpr int R0 = W * DEPTH;
constexpr int NPOS = ((NU + 2 * R0 + 63) / 64) * 64;
constexpr int NSLOT = 2;
#define CFG_TV 0
constexpr int THR_K = 0x7fffffff, THR_V = CFG_TV >= 16 ? 0x7fffffff : CFG_TV * 4100 * UPP / 16;
constexpr int WN = (2 * DEPTH - 1) * NL;
constexpr int REC = 8 + 2 * D;
constexpr int NREC = 2 * W + NSLOT * W + 1;
constexpr int NTHR = (W + 1) * 64;

constexpr int L_CTL = 0;
constexpr int L_LIST = 256;
constexpr int L_Q = L_LIST + NPOS * 8;
constexpr int L_BT = L_Q + 33 * 1024;
constexpr int L_REC = L_BT + B * NBPS * 4;
constexpr int L_END = L_REC + NREC * REC * 4;
constexpr int C_READY = 0, C_NTOT = 1, C_SVC = 2, C_QRDY = 3, C_PROG = 8;

constexpr size_t WS_EPOCH = 0;
constexpr size_t WS_FLAG = 4096;
constexpr size_t WS_PAY = 4096 + 65536;
constexpr int PAY_STRIDE = 2176;

using bf16x2 = __attribute__((ext_vector_type(2))) __bf16;
using u32x4 = __attribute__((ext_vector_type(4))) unsigned;
using f32x4 = __attribute__((ext_vector_type(4))) float;
using f32x2 = __attribute__((ext_vector_type(2))) float;
using i32x2 = __attribute__((ext_vector_type(2))) int;
typedef unsigned long long u64;
template <int N> struct IC { static constexpr int value = N; };
#define GAS __attribute__((address_space(1)))
#define LAS __attribute__((address_space(3)))
#define RLX_AGENT __ATOMIC_RELAXED, __HIP_MEMORY_SCOPE_AGENT
#define RLX_WG __ATOMIC_RELAXED, __HIP_MEMORY_SCOPE_WORKGROUP

__device__ __forceinline__ float dot2(unsigned k, unsigned q, float acc) {
  return __builtin_amdgcn_fdot2_f32_bf16(__builtin_bit_cast(bf16x2, k), __builtin_bit_cast(bf16x2, q), acc, false);
}
template <int CTRL> __device__ __forceinline__ float dpp(float x) {
  return __builtin_bit_cast(float, __builtin_amdgcn_mov_dpp(__builtin_bit_cast(int, x), CTRL, 0xf, 0xf, true));
}
template <int CTRL> __device__ __forceinline__ int dppi(int x) {
  return __builtin_amdgcn_update_dpp(0, x, CTRL, 0xf, 0xf, true);
}
constexpr int XOR1 = 0xB1, XOR2 = 0x4E, XOR7 = 0x141, XOR8 = 0x128, ROR4 = 0x124, NEWBC = 0x150;

__device__ __forceinline__ float sum16swap(float a, float b) {
  auto s = __builtin_amdgcn_permlane16_swap(__float_as_uint(a), __float_as_uint(b), false, false);
  return __uint_as_float(s[0]) + __uint_as_float(s[1]);
}
__device__ __forceinline__ float sum32swap(float a, float b) {
  auto s = __builtin_amdgcn_permlane32_swap(__float_as_uint(a), __float_as_uint(b), false, false);
  return __uint_as_float(s[0]) + __uint_as_float(s[1]);
}
__device__ __forceinline__ float max16(float a) {
  auto s = __builtin_amdgcn_permlane16_swap(__float_as_uint(a), __float_as_uint(a), false, false);
  return fmaxf(__uint_as_float(s[0]), __uint_as_float(s[1]));
}
__device__ __forceinline__ float max32(float a) {
  auto s = __builtin_amdgcn_permlane32_swap(__float_as_uint(a), __float_as_uint(a), false, false);
  return fmaxf(__uint_as_float(s[0]), __uint_as_float(s[1]));
}
__device__ __forceinline__ int lds_ld(LAS int* p) { return __hip_atomic_load(p, RLX_WG); }
__device__ __forceinline__ void lds_st(LAS int* p, int v) { __hip_atomic_store(p, v, RLX_WG); }
#define LGKM0() asm volatile("s_waitcnt lgkmcnt(0)" ::: "memory")
#define VM0() asm volatile("s_waitcnt vmcnt(0)" ::: "memory")
#define ST(k) do {} while (0)

__device__ __forceinline__ int scan32(int x) {
  x += dppi<0x111>(x); x += dppi<0x112>(x); x += dppi<0x114>(x); x += dppi<0x118>(x);
  const int r0 = __builtin_amdgcn_readlane(x, 15);
  return x + (((threadIdx.x & 63) >= 16) ? r0 : 0);
}

__global__ void __launch_bounds__(NTHR, (W + 4) / 4) attn_kernel(const uint16_t* __restrict__ q, const char* __restrict__ kc,
                                                    const char* __restrict__ vc, const int* __restrict__ bt,
                                                    const int* __restrict__ lens, uint16_t* __restrict__ out,
                                                    char* __restrict__ ws) {
  __shared__ __attribute__((aligned(16))) char smem[L_END];
  LAS char* sm = (LAS char*)smem;
  LAS int* ctl = (LAS int*)(sm + L_CTL);
  LAS float* recs = (LAS float*)(sm + L_REC);
  const int b = blockIdx.x >> 3, h = blockIdx.x & 7;
  const int tid = threadIdx.x, lane = tid & 63, wave = __builtin_amdgcn_readfirstlane(tid >> 6);
  const int r = lane >> 5, c = lane & 31;
  constexpr float C2 = 0.0625f * 1.4426950408889634f;
  ST(0);

  if (wave < W) {
    const int w = wave;
    const int len_raw = lens[b];
    int ph0[DEPTH];
#pragma unroll
    for (int k = 0; k < DEPTH; ++k) { const int u = w + W * k; ph0[k] = __builtin_amdgcn_readfirstlane(bt[b * NBPS + u / UPP] * UPP + (u % UPP)); }
    u32x4 qv[2];
    {
      const uint16_t* qp = q + (size_t)(b * HQ + h * 2) * D + 8 * c;
      asm volatile("global_load_dwordx4 %0, %2, off\n\tglobal_load_dwordx4 %1, %2, off offset:512" : "=&v"(qv[0]), "=&v"(qv[1]) : "v"(qp) : "memory");
    }
    if (lane < 2 + NSLOT) {
      const int ri = lane < 2 ? lane * W + w : 2 * W + (lane - 2) * W + w;
      LAS float* rec = recs + ri * REC;
      rec[0] = -INFINITY; rec[1] = 0.f; rec[2] = -INFINITY; rec[3] = 0.f; ((LAS int*)rec)[4] = -1;
    }
    if (lane == 0) lds_st(ctl + C_PROG + w, 0);
    LGKM0();
    __builtin_amdgcn_s_barrier();
    const bool uni = len_raw <= 0;
    const int len = uni ? SKV : min(len_raw, SKV);
    const int voff = r * 4096 + h * 512 + c * 16;
    u32x4 K[DEPTH][NL], V[DEPTH][NL];
    int meta[DEPTH];
    static_assert(NL == 8, "asm issue block is written for 8 loads");
#define LD8(POL) \
      "buffer_load_dwordx4 %0, %8, %9, 0 offen" POL "\n\t" \
      "buffer_load_dwordx4 %1, %8, %9, %10 offen" POL "\n\t" \
      "buffer_load_dwordx4 %2, %8, %9, %11 offen" POL "\n\t" \
      "buffer_load_dwordx4 %3, %8, %9, %12 offen" POL "\n\t" \
      "buffer_load_dwordx4 %4, %8, %9, %13 offen" POL "\n\t" \
      "buffer_load_dwordx4 %5, %8, %9, %14 offen" POL "\n\t" \
      "buffer_load_dwordx4 %6, %8, %9, %15 offen" POL "\n\t" \
      "buffer_load_dwordx4 %7, %8, %9, %16 offen" POL "\n\t"
#define ISSUE(BUF, base_, s_, phys_, rem_) do { \
      const u64 a_ = (u64)((base_) + (size_t)(unsigned)(phys_) * (UNIT * 4096)); \
      const u32x4 rs_ = {(unsigned)a_, (unsigned)(a_ >> 32), (unsigned)(rem_) * 4096u, 0x00020000u}; \
      if constexpr (THR_##BUF == 0x7fffffff || THR_##BUF == 0) { \
        if constexpr (THR_##BUF == 0) \
          asm volatile("s_nop 4\n\t" LD8("") \
            : "+v"(BUF[s_][0]), "+v"(BUF[s_][1]), "+v"(BUF[s_][2]), "+v"(BUF[s_][3]), "+v"(BUF[s_][4]), "+v"(BUF[s_][5]), "+v"(BUF[s_][6]), "+v"(BUF[s_][7]) \
            : "v"(voff), "s"(rs_), "s"(8192), "s"(2 * 8192), "s"(3 * 8192), "s"(4 * 8192), "s"(5 * 8192), "s"(6 * 8192), "s"(7 * 8192) : "memory"); \
        else \
          asm volatile("s_nop 4\n\t" LD8(" nt") \
            : "+v"(BUF[s_][0]), "+v"(BUF[s_][1]), "+v"(BUF[s_][2]), "+v"(BUF[s_][3]), "+v"(BUF[s_][4]), "+v"(BUF[s_][5]), "+v"(BUF[s_][6]), "+v"(BUF[s_][7]) \
            : "v"(voff), "s"(rs_), "s"(8192), "s"(2 * 8192), "s"(3 * 8192), "s"(4 * 8192), "s"(5 * 8192), "s"(6 * 8192), "s"(7 * 8192) : "memory"); \
      } else { \
        asm volatile("s_nop 2\n\ts_cmp_ge_u32 %17, %18\n\ts_cbranch_scc1 1f\n\t" LD8(" nt") "s_branch 2f\n\t1:\n\t" LD8("") "2:" \
            : "+v"(BUF[s_][0]), "+v"(BUF[s_][1]), "+v"(BUF[s_][2]), "+v"(BUF[s_][3]), "+v"(BUF[s_][4]), "+v"(BUF[s_][5]), "+v"(BUF[s_][6]), "+v"(BUF[s_][7]) \
            : "v"(voff), "s"(rs_), "s"(8192), "s"(2 * 8192), "s"(3 * 8192), "s"(4 * 8192), "s"(5 * 8192), "s"(6 * 8192), "s"(7 * 8192), \
              "s"(phys_), "i"(THR_##BUF) : "memory", "scc"); \
      } } while (0)
#define WAITH(BUF, s_, h_, N_) asm volatile("s_waitcnt vmcnt(%4)" : "+v"(BUF[s_][4 * (h_)]), "+v"(BUF[s_][4 * (h_) + 1]), "+v"(BUF[s_][4 * (h_) + 2]), "+v"(BUF[s_][4 * (h_) + 3]) : "i"(N_) : "memory")
#pragma unroll
    for (int k = 0; k < DEPTH; ++k)
#pragma unroll
      for (int t = 0; t < NL; ++t) asm volatile("" : "=v"(K[k][t]), "=v"(V[k][t]));
#pragma unroll
    for (int k = 0; k < DEPTH; ++k) {
      const int u = w + W * k;
      const int rem = __builtin_amdgcn_readfirstlane(max(0, min(UNIT, len - u * UNIT)));
      meta[k] = rem;
      ISSUE(K, kc, k, ph0[k], rem);
      ISSUE(V, vc, k, ph0[k], rem);
    }
    ST(1);
    asm volatile("s_waitcnt vmcnt(%2)" : "+v"(qv[0]), "+v"(qv[1]) : "i"(2 * DEPTH * NL) : "memory");
    if (uni) { qv[0] = u32x4{0u, 0u, 0u, 0u}; qv[1] = qv[0]; }

    float m = -INFINITY, l = 0.f, acc[16];
#pragma unroll
    for (int k = 0; k < 16; ++k) acc[k] = 0.f;
    int cur = 0;
    while (lds_ld(ctl + C_READY) == 0) __builtin_amdgcn_s_sleep(1);
    asm volatile("" ::: "memory");
    ST(2);
    const int ntot = __builtin_amdgcn_readfirstlane(lds_ld(ctl + C_NTOT));
    const int cnt = (ntot - w + W - 1) / W;

    auto dump = [&](int seg, int next) __attribute__((always_inline)) {
      int ri;
      if (seg == 0) ri = w;
      else if (seg == 255) ri = W + w;
      else {
        ri = 2 * W + ((seg - 1) % NSLOT) * W + w;
        while (lds_ld(ctl + C_SVC) < seg - NSLOT) __builtin_amdgcn_s_sleep(1);
        asm volatile("" ::: "memory");
      }
      LAS float* rec = recs + ri * REC;
      float o[8];
#pragma unroll
      for (int k = 0; k < 8; ++k) o[k] = sum32swap(acc[k], acc[8 + k]);
      float t = l;
      if constexpr (UNIT == 16) t += dpp<ROR4>(t);
      t += dpp<XOR8>(t); t = sum16swap(t, t); t = sum32swap(t, t);
      *(LAS f32x4*)(rec + 8 + lane * 8) = f32x4{o[0], o[1], o[2], o[3]};
      *(LAS f32x4*)(rec + 8 + lane * 8 + 4) = f32x4{o[4], o[5], o[6], o[7]};
      constexpr int G1 = UNIT == 16 ? 2 : 4;
      if (lane == 0 || lane == G1) *(LAS f32x2*)(rec + (lane ? 2 : 0)) = f32x2{m, t};
      if (lane == 0) ((LAS int*)rec)[4] = seg;
      LGKM0();
      if (lane == 0) lds_st(ctl + C_PROG + w, next);
    };

    int i = 0;
    auto stage = [&](auto sc_) __attribute__((always_inline)) {
      {
        constexpr int s = decltype(sc_)::value;
        const i32x2 ent = *(LAS i32x2*)(sm + L_LIST + (w + W * (i + DEPTH)) * 8);
        const int rem = meta[s] & 255, seg = meta[s] >> 8;
        if (seg != cur) {
          dump(cur, seg);
          m = -INFINITY; l = 0.f;
#pragma unroll
          for (int k = 0; k < 16; ++k) acc[k] = 0.f;
          const int qi = seg == 255 ? 0 : seg;
          while (lds_ld(ctl + C_QRDY) == 0) __builtin_amdgcn_s_sleep(1);
          asm volatile("" ::: "memory");
          qv[0] = *(LAS u32x4*)(sm + L_Q + qi * 1024 + c * 16);
          qv[1] = *(LAS u32x4*)(sm + L_Q + qi * 1024 + 512 + c * 16);
          cur = seg;
        }
        float x[2 * NL];
#pragma unroll
        for (int t = 0; t < NL; ++t) {
          if (t == 0) WAITH(K, s, 0, WN + 4);
          if (t == 4) WAITH(K, s, 1, WN);
#pragma unroll
          for (int g = 0; g < 2; ++g) {
            float d = 0.f;
#pragma unroll
            for (int k = 0; k < 4; ++k) d = dot2(K[s][t][k], qv[g][k], d);
            x[t * 2 + g] = d;
          }
        }
        const int nphys = __builtin_amdgcn_readfirstlane(ent.x), nmeta = __builtin_amdgcn_readfirstlane(ent.y);
        ISSUE(K, kc, s, nphys, nmeta & 255);
#pragma unroll
        for (int hh = 0; hh < NL; ++hh) x[hh] = sum16swap(x[hh], x[hh + NL]);
#define TR_STEP(HALF, CTRL, BIT) _Pragma("unroll") for (int hh = 0; hh < (HALF); ++hh) { \
          const float lo_ = x[hh], hi_ = x[hh + (HALF)]; const float t1_ = lo_ + dpp<CTRL>(lo_), t2_ = hi_ + dpp<CTRL>(hi_); \
          x[hh] = (c & (BIT)) ? t2_ : t1_; }
        float sc; int tok;
        if constexpr (UNIT == 16) {
          TR_STEP(4, XOR8, 8) TR_STEP(2, XOR7, 4) TR_STEP(1, XOR2, 2)
          sc = x[0] + dpp<XOR1>(x[0]);
          tok = 2 * (c >> 2) + r;
        } else {
          TR_STEP(2, XOR8, 8) TR_STEP(1, XOR7, 4)
          sc = x[0] + dpp<XOR2>(x[0]);
          sc = sc + dpp<XOR1>(sc);
          tok = 2 * (c >> 3) + r;
        }
#undef TR_STEP
        sc = (tok < rem) ? sc * C2 : -INFINITY;
        float pm = sc;
        if constexpr (UNIT == 16) pm = fmaxf(pm, dpp<ROR4>(pm));
        pm = fmaxf(pm, dpp<XOR8>(pm)); pm = max16(pm); pm = max32(pm);
        const float mn = fmaxf(m, pm), msub = (mn == -INFINITY) ? 0.f : mn;
        const float alpha = __builtin_amdgcn_exp2f(m - msub), p = __builtin_amdgcn_exp2f(sc - msub);
        l = fmaf(l, alpha, p); m = mn;
        const float a0 = __builtin_bit_cast(float, __builtin_amdgcn_readlane(__builtin_bit_cast(int, alpha), 0));
        const float a1 = __builtin_bit_cast(float, __builtin_amdgcn_readlane(__builtin_bit_cast(int, alpha), UNIT == 16 ? 2 : 4));
        if (__builtin_bit_cast(int, a0) != 0x3f800000 || __builtin_bit_cast(int, a1) != 0x3f800000) {
#pragma unroll
          for (int k = 0; k < 8; ++k) { acc[k] *= a0; acc[8 + k] *= a1; }
        }
        auto ps = __builtin_amdgcn_permlane16_swap(__float_as_uint(p), __float_as_uint(p), false, false);
        const float pl = __uint_as_float(ps[0]), phh = __uint_as_float(ps[1]);
#define PB(idx_) ((idx_) < NL ? dpp<NEWBC + ((idx_) % NL) * (16 / NL)>(pl) : dpp<NEWBC + ((idx_) % NL) * (16 / NL)>(phh))
#define PV_T(t_) do { float vf[8]; \
          _Pragma("unroll") for (int k = 0; k < 4; ++k) { vf[2 * k] = __builtin_bit_cast(float, V[s][t_][k] << 16); vf[2 * k + 1] = __builtin_bit_cast(float, V[s][t_][k] & 0xffff0000u); } \
          const float p0_ = PB(2 * (t_)), p1_ = PB(2 * (t_) + 1); \
          _Pragma("unroll") for (int k = 0; k < 8; ++k) { acc[k] = fmaf(p0_, vf[k], acc[k]); acc[8 + k] = fmaf(p1_, vf[k], acc[8 + k]); } } while (0)
        WAITH(V, s, 0, WN + 4);
        PV_T(0); PV_T(1); PV_T(2); PV_T(3);
        WAITH(V, s, 1, WN);
        PV_T(4); PV_T(5); PV_T(6); PV_T(7);
#undef PV_T
#undef PB
        ISSUE(V, vc, s, nphys, nmeta & 255);
        meta[s] = nmeta;
        ++i;
      }
    };
    for (int gidx = cnt / DEPTH; gidx > 0; --gidx) {
      stage(IC<0>{});
      if constexpr (DEPTH > 1) stage(IC<1 % DEPTH>{});
      if constexpr (DEPTH > 2) stage(IC<2 % DEPTH>{});
      if constexpr (DEPTH > 3) stage(IC<3 % DEPTH>{});
    }
    if constexpr (DEPTH > 1) {
      if (i < cnt) {
        stage(IC<0>{});
        if constexpr (DEPTH > 2) {
          if (i < cnt) {
            stage(IC<1 % DEPTH>{});
            if constexpr (DEPTH > 3) { if (i < cnt) stage(IC<2 % DEPTH>{}); }
          }
        }
      }
    }
#pragma unroll
    for (int k = 0; k < DEPTH; ++k) { WAITH(K, k, 0, 0); WAITH(K, k, 1, 0); WAITH(V, k, 0, 0); WAITH(V, k, 1, 0); }
#undef ISSUE
    ST(4);
    dump(cur, 1000);
  } else {
    const int lraw = lens[lane & 31];
    u32x4 btr[16];
#pragma unroll
    for (int k = 0; k < 16; ++k) btr[k] = *reinterpret_cast<const u32x4*>(bt + k * 256 + lane * 4);
    const u64 ep = __hip_atomic_load((GAS u64*)(ws + WS_EPOCH) + h * B + (lane & 31), RLX_AGENT);
    if (lane == 0) { lds_st(ctl + C_READY, 0); lds_st(ctl + C_SVC, 0); lds_st(ctl + C_QRDY, 0); }
    if (lane == 0) { LAS float* rec = recs + (NREC - 1) * REC; rec[0] = -INFINITY; rec[1] = 0.f; rec[2] = -INFINITY; rec[3] = 0.f; }
    LGKM0();
    __builtin_amdgcn_s_barrier();
    ST(1);
    const bool luni = lraw <= 0;
    const int llen = luni ? SKV : min(lraw, SKV);
    const int n = lane < 32 ? (llen + UNIT - 1) / UNIT : 0;
    const int P = __builtin_amdgcn_readlane(scan32(n), 31);
    const int T = max((P + B - 1) / B, R0);
    const int e = max(n - T, 0), sp = lane < 32 ? max(T - n, 0) : 0;
    const int sc = scan32(e | (sp << 16));
    const int Ein = sc & 0xffff, Sin = sc >> 16;
    const int E0 = Ein - e, S0 = Sin - sp;
    const int Etot = __builtin_amdgcn_readlane(Ein, 31);
    const int my_n = __builtin_amdgcn_readlane(n, b), my_len = __builtin_amdgcn_readlane(llen, b);
    const int hs = __builtin_amdgcn_readlane(S0, b), he = min(__builtin_amdgcn_readlane(Sin, b), Etot);
    const int nh = max(he - hs, 0);
    const int n_own = min(my_n, T);
    const int ntot = R0 + nh + max(0, n_own - R0);
    const int plo = max(E0, hs), phi = min(Ein, he);
    const u64 pmask = __ballot(lane < 32 && phi > plo);
    const int myE0 = __builtin_amdgcn_readlane(E0, b), myE1 = __builtin_amdgcn_readlane(Ein, b);
    const u64 hmask = __ballot(lane < 32 && min(min(Sin, Etot), myE1) > max(S0, myE0));
    const u64 omask = __ballot(e > 0), smask = __ballot(sp > 0);
    constexpr int NP = NPOS / 64;
    int src[NP], un[NP], sg[NP], ln[NP];
#pragma unroll
    for (int k = 0; k < NP; ++k) {
      const int j = lane + 64 * k;
      src[k] = b; ln[k] = my_len;
      un[k] = j < R0 ? j : j - nh;
      sg[k] = (j < R0 || nh == 0) ? 0 : 255;
    }
    {
      u64 mk = pmask; int seg = 1;
      while (mk) {
        const int bp = __builtin_ctzll(mk); mk &= mk - 1;
        const int lo = __builtin_amdgcn_readlane(plo, bp), hi = __builtin_amdgcn_readlane(phi, bp);
        const int e0 = __builtin_amdgcn_readlane(E0, bp), bl = __builtin_amdgcn_readlane(llen, bp);
#pragma unroll
        for (int k = 0; k < NP; ++k) {
          const int xq = lane + 64 * k - R0 + hs;
          const bool in = xq >= lo && xq < hi;
          src[k] = in ? bp : src[k]; un[k] = in ? T + xq - e0 : un[k]; sg[k] = in ? seg : sg[k]; ln[k] = in ? bl : ln[k];
        }
        ++seg;
      }
    }
    ST(2);
    int phys[NP];
#pragma unroll
    for (int k = 0; k < 16; ++k) *(LAS u32x4*)(sm + L_BT + k * 1024 + lane * 16) = btr[k];
    LGKM0();
#pragma unroll
    for (int k = 0; k < NP; ++k) phys[k] = *(LAS int*)(sm + L_BT + (src[k] * NBPS + min(un[k] / UPP, NBPS - 1)) * 4);
#pragma unroll
    for (int k = 0; k < NP; ++k) {
      const int j = lane + 64 * k;
      const int rem = j < ntot ? max(0, min(UNIT, ln[k] - un[k] * UNIT)) : 0;
      *(LAS i32x2*)(sm + L_LIST + j * 8) = i32x2{phys[k] * UPP + (un[k] % UPP), rem | (sg[k] << 8)};
    }
    if (lane == 0) lds_st(ctl + C_NTOT, ntot);
    LGKM0();
    if (lane == 0) lds_st(ctl + C_READY, 1);
    ST(3);
    if (pmask) {
    {
      u32x4 q0 = *reinterpret_cast<const u32x4*>(q + (size_t)(b * HQ + h * 2) * D + 8 * lane);
      u64 mk = pmask;
      u32x4 qq[3]; int bb[3];
#pragma unroll
      for (int k = 0; k < 3; ++k) {
        bb[k] = mk ? __builtin_ctzll(mk) : b; mk &= mk - 1;
        qq[k] = *reinterpret_cast<const u32x4*>(q + (size_t)(bb[k] * HQ + h * 2) * D + 8 * lane);
      }
      const u64 umask = __ballot(luni);
      const u32x4 z = {0u, 0u, 0u, 0u};
      *(LAS u32x4*)(sm + L_Q + lane * 16) = ((umask >> b) & 1) ? z : q0;
#pragma unroll
      for (int k = 0; k < 3; ++k) *(LAS u32x4*)(sm + L_Q + (k + 1) * 1024 + lane * 16) = ((umask >> bb[k]) & 1) ? z : qq[k];
      int seg = 4;
      while (mk) {
        const int bp = __builtin_ctzll(mk); mk &= mk - 1;
        const u32x4 qx = *reinterpret_cast<const u32x4*>(q + (size_t)(bp * HQ + h * 2) * D + 8 * lane);
        *(LAS u32x4*)(sm + L_Q + seg * 1024 + lane * 16) = ((umask >> bp) & 1) ? z : qx;
        ++seg;
      }
    }
      LGKM0();
      if (lane == 0) lds_st(ctl + C_QRDY, 1);
    }
    ST(4);

    const u64 tagv = ep * 6364136223846793005ull + 1442695040888963407ull;
    {
      u64 mk = pmask; int seg = 1;
      while (mk) {
        const int bp = __builtin_ctzll(mk); mk &= mk - 1;
        for (;;) {
          const int pr = lane < W ? lds_ld(ctl + C_PROG + lane) : 1000;
          if (__all(pr > seg)) break;
          __builtin_amdgcn_s_sleep(4);
        }
        asm volatile("" ::: "memory");
        LAS float* rb = recs + (2 * W + ((seg - 1) % NSLOT) * W) * REC;
        float M = -INFINITY, mw[W], lw[W];
#pragma unroll
        for (int k = 0; k < W; ++k) {
          const bool ok = ((LAS int*)(rb + k * REC))[4] == seg;
          mw[k] = ok ? rb[k * REC + 2 * r] : -INFINITY; lw[k] = ok ? rb[k * REC + 2 * r + 1] : 0.f;
          M = fmaxf(M, mw[k]);
        }
        float L = 0.f; f32x4 o0 = {0.f, 0.f, 0.f, 0.f}, o1 = o0;
#pragma unroll
        for (int k = 0; k < W; ++k) {
          const bool ok = lw[k] > 0.f;
          const float wt = ok ? __builtin_amdgcn_exp2f(mw[k] - M) : 0.f;
          f32x4 a0 = *(LAS f32x4*)(rb + k * REC + 8 + lane * 8), a1 = *(LAS f32x4*)(rb + k * REC + 8 + lane * 8 + 4);
          const f32x4 z = {0.f, 0.f, 0.f, 0.f};
          a0 = ok ? a0 : z; a1 = ok ? a1 : z;
          L = fmaf(wt, lw[k], L); o0 += wt * a0; o1 += wt * a1;
        }
        LGKM0();
        if (lane == 0) lds_st(ctl + C_SVC, seg);
        const int slot = (h * B + bp) * B + b;
        const int pid = h * 64 + __builtin_popcountll(omask & ((1ull << bp) - 1)) + __builtin_popcountll(smask & ((1ull << b) - 1));
        const __amdgpu_buffer_rsrc_t rs = __builtin_amdgcn_make_buffer_rsrc((void*)(ws + WS_PAY + (size_t)pid * PAY_STRIDE), 0, PAY_STRIDE, 0x00020000);
        __builtin_amdgcn_raw_buffer_store_b128(__builtin_bit_cast(u32x4, o0), rs, 128 + lane * 32, 0, 16);
        __builtin_amdgcn_raw_buffer_store_b128(__builtin_bit_cast(u32x4, o1), rs, 128 + lane * 32 + 16, 0, 16);
        if (c == 0) __builtin_amdgcn_raw_buffer_store_b64(__builtin_bit_cast(__attribute__((ext_vector_type(2))) unsigned, f32x2{M, L}), rs, r * 8, 0, 16);
        VM0();
        const unsigned tlo = __builtin_amdgcn_readlane((unsigned)tagv, bp), thi = __builtin_amdgcn_readlane((unsigned)(tagv >> 32), bp);
        if (lane == 0) __hip_atomic_store((GAS u64*)(ws + WS_FLAG) + slot, ((u64)thi << 32) | tlo, RLX_AGENT);
        ++seg;
      }
    }
    if (hmask) {
      const unsigned tlo = __builtin_amdgcn_readlane((unsigned)tagv, b), thi = __builtin_amdgcn_readlane((unsigned)(tagv >> 32), b);
      const u64 tag = ((u64)thi << 32) | tlo;
      const bool mine = (hmask >> lane) & 1;
      for (unsigned spins = 0; spins < (1u << 22); ++spins) {
        const u64 f = __hip_atomic_load((GAS u64*)(ws + WS_FLAG) + (h * B + b) * B + (lane & 31), RLX_AGENT);
        if (__all(!mine || f == tag)) break;
        __builtin_amdgcn_s_sleep(8);
      }
      __builtin_amdgcn_fence(__ATOMIC_ACQUIRE, "agent");
      float M = -INFINITY, L = 0.f; f32x4 o0 = {0.f, 0.f, 0.f, 0.f}, o1 = o0;
      u64 mk = hmask;
      while (mk) {
        const int hb = __builtin_ctzll(mk); mk &= mk - 1;
        const int pid = h * 64 + __builtin_popcountll(omask & ((1ull << b) - 1)) + __builtin_popcountll(smask & ((1ull << hb) - 1));
        const __amdgpu_buffer_rsrc_t rs = __builtin_amdgcn_make_buffer_rsrc((void*)(ws + WS_PAY + (size_t)pid * PAY_STRIDE), 0, PAY_STRIDE, 0x00020000);
        const f32x4 a0 = __builtin_bit_cast(f32x4, __builtin_amdgcn_raw_buffer_load_b128(rs, 128 + lane * 32, 0, 16));
        const f32x4 a1 = __builtin_bit_cast(f32x4, __builtin_amdgcn_raw_buffer_load_b128(rs, 128 + lane * 32 + 16, 0, 16));
        const f32x2 ml = __builtin_bit_cast(f32x2, __builtin_amdgcn_raw_buffer_load_b64(rs, r * 8, 0, 16));
        const float Mn = fmaxf(M, ml.x);
        const float wo = (L > 0.f) ? __builtin_amdgcn_exp2f(M - Mn) : 0.f, wn = (ml.y > 0.f) ? __builtin_amdgcn_exp2f(ml.x - Mn) : 0.f;
        L = L * wo + ml.y * wn; o0 = o0 * wo + a0 * wn; o1 = o1 * wo + a1 * wn; M = Mn;
      }
      LAS float* rec = recs + (NREC - 1) * REC;
      *(LAS f32x4*)(rec + 8 + lane * 8) = o0;
      *(LAS f32x4*)(rec + 8 + lane * 8 + 4) = o1;
      if (c == 0) *(LAS f32x2*)(rec + 2 * r) = f32x2{M, L};
      if (lane == 0) __hip_atomic_store((GAS u64*)(ws + WS_EPOCH) + h * B + b, tag, RLX_AGENT);
    }
  }
  ST(5);
  LGKM0();
  __builtin_amdgcn_s_barrier();
  asm volatile("" ::: "memory");
  ST(6);
  if (tid < 256) {
    const int g = tid >> 7, d0 = 2 * (tid & 127);
    constexpr int NF = 2 * W + 1;
    float mw[NF], lw[NF], M = -INFINITY;
#pragma unroll
    for (int k = 0; k < NF; ++k) {
      const int ri = k < 2 * W ? k : NREC - 1;
      const f32x2 ml = *(LAS f32x2*)(recs + ri * REC + 2 * g);
      mw[k] = ml.x; lw[k] = ml.y; M = fmaxf(M, ml.x);
    }
    float L = 0.f, o0 = 0.f, o1 = 0.f;
#pragma unroll
    for (int k = 0; k < NF; ++k) {
      const int ri = k < 2 * W ? k : NREC - 1;
      const bool ok = lw[k] > 0.f;
      const float wt = ok ? __builtin_amdgcn_exp2f(mw[k] - M) : 0.f;
      const f32x2 a = *(LAS f32x2*)(recs + ri * REC + 8 + g * D + d0);
      L = fmaf(wt, lw[k], L); o0 = fmaf(wt, ok ? a.x : 0.f, o0); o1 = fmaf(wt, ok ? a.y : 0.f, o1);
    }
    const float inv = 1.f / L;
    __hip_bfloat162 o; o.x = __float2bfloat16(o0 * inv); o.y = __float2bfloat16(o1 * inv);
    *reinterpret_cast<__hip_bfloat162*>(out + (size_t)(b * HQ + h * 2 + g) * D + d0) = o;
  }
}

extern "C" void kernel_launch(void* const* d_in, const int* in_sizes, int n_in, void* d_out, int out_size, void* d_ws,
                              size_t ws_size, hipStream_t stream) {
  attn_kernel<<<B * HKV, NTHR, 0, stream>>>((const uint16_t*)d_in[0], (const char*)d_in[1], (const char*)d_in[2],
                                            (const int*)d_in[3], (const int*)d_in[4], (uint16_t*)d_out, (char*)d_ws);
}
```

```cpp
#include <hip/hip_runtime.h>
#include <hip/hip_bf16.h>
#include <cstdint>

constexpr int B = 32, HQ = 16, HKV = 8, D = 256, PAGE = 16, NBPS = 128, SKV = NBPS * PAGE;
constexpr int W = 4;
constexpr int NL = 8;
constexpr int NPRE = 4;
constexpr int R1 = NPRE * W;
constexpr int NPOS = 192;
constexpr int NSLOT = 2;
constexpr int REC = 8 + 2 * D;
constexpr int NREC = W + 1 + NSLOT * W;
constexpr int RI_SVC = W, RI_RING = W + 1;
constexpr int NTHR = (W + 1) * 64;
constexpr int SEG_OWN2 = 255, PROG_DONE = 1000;
static_assert(R1 + NBPS + W <= NPOS && NPOS % 64 == 0 && W * 64 == 256, "list capacity / merge mapping");

constexpr int L_CTL = 0;
constexpr int L_LIST = 256;
constexpr int L_Q = L_LIST + NPOS * 8;
constexpr int L_BT = L_Q + 32 * 1024;
constexpr int L_REC = L_BT + B * NBPS * 4;
constexpr int L_END = L_REC + NREC * REC * 4;
constexpr int C_READY = 0, C_NTOT = 1, C_SVC = 2, C_QRDY = 3, C_PROG = 8;

constexpr size_t WS_EPOCH = 0;
constexpr size_t WS_FLAG = 4096;
constexpr size_t WS_PAY = 4096 + 65536;
constexpr int PAY_STRIDE = 2176;

using bf16x2 = __attribute__((ext_vector_type(2))) __bf16;
using u32x4 = __attribute__((ext_vector_type(4))) unsigned;
using u32x2 = __attribute__((ext_vector_type(2))) unsigned;
using f32x4 = __attribute__((ext_vector_type(4))) float;
using f32x2 = __attribute__((ext_vector_type(2))) float;
using i32x2 = __attribute__((ext_vector_type(2))) int;
typedef unsigned long long u64;
#define GAS __attribute__((address_space(1)))
#define LAS __attribute__((address_space(3)))
#define RLX_AGENT __ATOMIC_RELAXED, __HIP_MEMORY_SCOPE_AGENT
#define RLX_WG __ATOMIC_RELAXED, __HIP_MEMORY_SCOPE_WORKGROUP
#define LGKM0() asm volatile("s_waitcnt lgkmcnt(0)" ::: "memory")
#define VM0() asm volatile("s_waitcnt vmcnt(0)" ::: "memory")
#define CFENCE() asm volatile("" ::: "memory")

__device__ __forceinline__ float dot2(unsigned k, unsigned q, float acc) {
  return __builtin_amdgcn_fdot2_f32_bf16(__builtin_bit_cast(bf16x2, k), __builtin_bit_cast(bf16x2, q), acc, false);
}
template <int CTRL> __device__ __forceinline__ float dpp(float x) {
  return __builtin_bit_cast(float, __builtin_amdgcn_mov_dpp(__builtin_bit_cast(int, x), CTRL, 0xf, 0xf, true));
}
template <int CTRL> __device__ __forceinline__ int dppi(int x) {
  return __builtin_amdgcn_update_dpp(0, x, CTRL, 0xf, 0xf, true);
}
constexpr int XOR1 = 0xB1, XOR2 = 0x4E, XOR7 = 0x141, XOR8 = 0x128, ROR4 = 0x124, NEWBC = 0x150;

__device__ __forceinline__ float sum16swap(float a, float b) {
  auto s = __builtin_amdgcn_permlane16_swap(__float_as_uint(a), __float_as_uint(b), false, false);
  return __uint_as_float(s[0]) + __uint_as_float(s[1]);
}
__device__ __forceinline__ float sum32swap(float a, float b) {
  auto s = __builtin_amdgcn_permlane32_swap(__float_as_uint(a), __float_as_uint(b), false, false);
  return __uint_as_float(s[0]) + __uint_as_float(s[1]);
}
__device__ __forceinline__ float max16(float a) {
  auto s = __builtin_amdgcn_permlane16_swap(__float_as_uint(a), __float_as_uint(a), false, false);
  return fmaxf(__uint_as_float(s[0]), __uint_as_float(s[1]));
}
__device__ __forceinline__ float max32(float a) {
  auto s = __builtin_amdgcn_permlane32_swap(__float_as_uint(a), __float_as_uint(a), false, false);
  return fmaxf(__uint_as_float(s[0]), __uint_as_float(s[1]));
}
__device__ __forceinline__ int lds_ld(LAS int* p) { return __hip_atomic_load(p, RLX_WG); }
__device__ __forceinline__ void lds_st(LAS int* p, int v) { __hip_atomic_store(p, v, RLX_WG); }
__device__ __forceinline__ int scan32(int x) {
  x += dppi<0x111>(x); x += dppi<0x112>(x); x += dppi<0x114>(x); x += dppi<0x118>(x);
  const int r0 = __builtin_amdgcn_readlane(x, 15);
  return x + (((threadIdx.x & 63) >= 16) ? r0 : 0);
}
__device__ __forceinline__ u64 readlane64(u64 v, int l) {
  return ((u64)(unsigned)__builtin_amdgcn_readlane((unsigned)(v >> 32), l) << 32) | (unsigned)__builtin_amdgcn_readlane((unsigned)v, l);
}

__global__ void __launch_bounds__(NTHR, 2) attn_kernel(const uint16_t* __restrict__ q, const char* __restrict__ kc,
                                                       const char* __restrict__ vc, const int* __restrict__ bt,
                                                       const int* __restrict__ lens, uint16_t* __restrict__ out,
                                                       char* __restrict__ ws) {
  __shared__ __attribute__((aligned(16))) char smem[L_END];
  LAS char* sm = (LAS char*)smem;
  LAS int* ctl = (LAS int*)(sm + L_CTL);
  LAS float* recs = (LAS float*)(sm + L_REC);
  const int b = blockIdx.x >> 3, h = blockIdx.x & 7;
  const int tid = threadIdx.x, lane = tid & 63, wave = __builtin_amdgcn_readfirstlane(tid >> 6);
  const int r = lane >> 5, c = lane & 31;
  constexpr float C2 = 0.0625f * 1.4426950408889634f;

  if (__builtin_expect(wave != W, 1)) {
    const int w = wave;
    if (lane < 1 + NSLOT) {
      const int ri = lane == 0 ? w : RI_RING + (lane - 1) * W + w;
      LAS float* rec = recs + ri * REC;
      rec[0] = -INFINITY; rec[1] = 0.f; rec[2] = -INFINITY; rec[3] = 0.f; ((LAS int*)rec)[4] = -1;
    }
    if (lane == 0) lds_st(ctl + C_PROG + w, 0);
    LGKM0();
    int len_raw, ph0[NPRE];
    asm volatile("s_load_dword %0, %5, 0x0\n\ts_load_dword %1, %6, 0x0\n\ts_load_dword %2, %6, %7\n\ts_load_dword %3, %6, %8\n\ts_load_dword %4, %6, %9"
                 : "=&s"(len_raw), "=&s"(ph0[0]), "=&s"(ph0[1]), "=&s"(ph0[2]), "=&s"(ph0[3])
                 : "s"(lens + b), "s"(bt + b * NBPS + w), "i"(W * 4), "i"(W * 8), "i"(W * 12) : "memory");
    u32x4 qv[2];
    {
      const uint16_t* qp = q + (size_t)(b * HQ + h * 2) * D + 8 * c;
      asm volatile("global_load_dwordx4 %0, %2, off\n\tglobal_load_dwordx4 %1, %2, off offset:512" : "=&v"(qv[0]), "=&v"(qv[1]) : "v"(qp) : "memory");
    }
    __builtin_amdgcn_s_barrier();
    asm volatile("s_waitcnt lgkmcnt(0)" : "+s"(len_raw), "+s"(ph0[0]), "+s"(ph0[1]), "+s"(ph0[2]), "+s"(ph0[3]) :: "memory");
    const bool uni = len_raw <= 0;
    const int len = uni ? SKV : min(len_raw, SKV);
    const int voff = r * 4096 + h * 512 + c * 16;
    u32x4 K[NL], V[NL];
#define ISSUE(BUF, base_, phys_, rem_) do { \
      const u64 a_ = (u64)((base_) + (size_t)(unsigned)(phys_) * (PAGE * 4096)); \
      const u32x4 rs_ = {(unsigned)a_, (unsigned)(a_ >> 32), (unsigned)(rem_) * 4096u, 0x00020000u}; \
      asm volatile("s_nop 4\n\t" \
          "buffer_load_dwordx4 %0, %8, %9, 0 offen nt\n\t" \
          "buffer_load_dwordx4 %1, %8, %9, %10 offen nt\n\t" \
          "buffer_load_dwordx4 %2, %8, %9, %11 offen nt\n\t" \
          "buffer_load_dwordx4 %3, %8, %9, %12 offen nt\n\t" \
          "buffer_load_dwordx4 %4, %8, %9, %13 offen nt\n\t" \
          "buffer_load_dwordx4 %5, %8, %9, %14 offen nt\n\t" \
          "buffer_load_dwordx4 %6, %8, %9, %15 offen nt\n\t" \
          "buffer_load_dwordx4 %7, %8, %9, %16 offen nt" \
          : "+v"(BUF[0]), "+v"(BUF[1]), "+v"(BUF[2]), "+v"(BUF[3]), "+v"(BUF[4]), "+v"(BUF[5]), "+v"(BUF[6]), "+v"(BUF[7]) \
          : "v"(voff), "s"(rs_), "s"(8192), "s"(2 * 8192), "s"(3 * 8192), "s"(4 * 8192), "s"(5 * 8192), "s"(6 * 8192), "s"(7 * 8192) : "memory"); \
    } while (0)
#define WAITB(BUF, N_) asm volatile("s_waitcnt vmcnt(%8)" \
          : "+v"(BUF[0]), "+v"(BUF[1]), "+v"(BUF[2]), "+v"(BUF[3]), "+v"(BUF[4]), "+v"(BUF[5]), "+v"(BUF[6]), "+v"(BUF[7]) \
          : "i"(N_) : "memory")
#pragma unroll
    for (int t = 0; t < NL; ++t) asm volatile("" : "=v"(K[t]), "=v"(V[t]));
    int meta = __builtin_amdgcn_readfirstlane(max(0, min(PAGE, len - w * PAGE)));
    ISSUE(K, kc, ph0[0], meta);
    ISSUE(V, vc, ph0[0], meta);
    if (lane < NPRE - 1) {
      const int u = w + W * (1 + lane);
      *(LAS i32x2*)(sm + L_LIST + u * 8) = i32x2{lane == 0 ? ph0[1] : lane == 1 ? ph0[2] : ph0[3], max(0, min(PAGE, len - u * PAGE))};
    }
    asm volatile("s_waitcnt vmcnt(%2)" : "+v"(qv[0]), "+v"(qv[1]) : "i"(2 * NL) : "memory");
    if (uni) { qv[0] = u32x4{0u, 0u, 0u, 0u}; qv[1] = qv[0]; }

    GAS bf16x2* op = (GAS bf16x2*)(out + (size_t)(b * HQ + h * 2) * D) + tid;
    asm volatile("" : "+v"(op));
    float m = -INFINITY, l = 0.f, acc[16];
#pragma unroll
    for (int k = 0; k < 16; ++k) acc[k] = 0.f;
    int cur = 0;
    int cnt = NPRE;

    auto dump = [&](int seg, int next) __attribute__((always_inline)) {
      int ri = w;
      if (seg != 0 && seg != SEG_OWN2) {
        ri = RI_RING + ((seg - 1) % NSLOT) * W + w;
        while (lds_ld(ctl + C_SVC) < seg - NSLOT) __builtin_amdgcn_s_sleep(1);
        CFENCE();
      }
      LAS float* rec = recs + ri * REC;
      float o[8];
#pragma unroll
      for (int k = 0; k < 8; ++k) o[k] = sum32swap(acc[k], acc[8 + k]);
      float t = l;
      t += dpp<ROR4>(t); t += dpp<XOR8>(t); t = sum16swap(t, t); t = sum32swap(t, t);
      *(LAS f32x4*)(rec + 8 + lane * 8) = f32x4{o[0], o[1], o[2], o[3]};
      *(LAS f32x4*)(rec + 8 + lane * 8 + 4) = f32x4{o[4], o[5], o[6], o[7]};
      if (lane < 3) *(LAS f32x2*)(rec + (lane == 1 ? 4 : lane)) = f32x2{lane == 1 ? __builtin_bit_cast(float, seg) : m, t};
      LGKM0();
      if (lane == 0) lds_st(ctl + C_PROG + w, next);
    };

    for (int i = 0; i < cnt; ++i) {
      if (i == NPRE - 1) {
        while (lds_ld(ctl + C_READY) == 0) __builtin_amdgcn_s_sleep(1);
        CFENCE();
        cnt = (__builtin_amdgcn_readfirstlane(lds_ld(ctl + C_NTOT)) - w + W - 1) / W;
      }
      const i32x2 ent = *(LAS i32x2*)(sm + L_LIST + (w + W * (i + 1)) * 8);
      const int rem = meta & 255, seg = meta >> 8;
      if (seg != cur) {
        dump(cur, seg);
        const int qi = seg == SEG_OWN2 ? 0 : seg;
        while (lds_ld(ctl + C_QRDY) == 0) __builtin_amdgcn_s_sleep(1);
        CFENCE();
        qv[0] = *(LAS u32x4*)(sm + L_Q + qi * 1024 + c * 16);
        qv[1] = *(LAS u32x4*)(sm + L_Q + qi * 1024 + 512 + c * 16);
        const bool own = seg == SEG_OWN2;
        LAS float* rec = recs + w * REC;
        const f32x2 ml = *(LAS f32x2*)(rec + (lane & 2));
        m = own ? ml.x : -INFINITY;
        l = (own && lane < 4) ? ml.y : 0.f;
#pragma unroll
        for (int k = 0; k < 4; ++k) {
          const f32x4 a = *(LAS f32x4*)(rec + 8 + (k >> 1) * D + c * 8 + (k & 1) * 4);
#pragma unroll
          for (int j = 0; j < 4; ++j) acc[4 * k + j] = (own && r == 0) ? a[j] : 0.f;
        }
        cur = seg;
      }
      WAITB(K, NL);
      float x[2 * NL];
#pragma unroll
      for (int t = 0; t < NL; ++t)
#pragma unroll
        for (int g = 0; g < 2; ++g) {
          float d = 0.f;
#pragma unroll
          for (int k = 0; k < 4; ++k) d = dot2(K[t][k], qv[g][k], d);
          x[t * 2 + g] = d;
        }
      const int nphys = __builtin_amdgcn_readfirstlane(ent.x), nmeta = __builtin_amdgcn_readfirstlane(ent.y);
      ISSUE(K, kc, nphys, nmeta & 255);
#pragma unroll
      for (int hh = 0; hh < 8; ++hh) x[hh] = sum16swap(x[hh], x[hh + 8]);
#define TR_STEP(HALF, CTRL, BIT) _Pragma("unroll") for (int hh = 0; hh < (HALF); ++hh) { \
        const float lo_ = x[hh], hi_ = x[hh + (HALF)]; const float t1_ = lo_ + dpp<CTRL>(lo_), t2_ = hi_ + dpp<CTRL>(hi_); \
        x[hh] = (c & (BIT)) ? t2_ : t1_; }
      TR_STEP(4, XOR8, 8) TR_STEP(2, XOR7, 4) TR_STEP(1, XOR2, 2)
#undef TR_STEP
      float sc = x[0] + dpp<XOR1>(x[0]);
      sc = (2 * (c >> 2) + r < rem) ? sc * C2 : -INFINITY;
      float pm = fmaxf(sc, dpp<ROR4>(sc));
      pm = fmaxf(pm, dpp<XOR8>(pm)); pm = max16(pm); pm = max32(pm);
      const float mn = fmaxf(m, pm), msub = (mn == -INFINITY) ? 0.f : mn;
      const float alpha = __builtin_amdgcn_exp2f(m - msub), p = __builtin_amdgcn_exp2f(sc - msub);
      l = fmaf(l, alpha, p); m = mn;
      const int a0 = __builtin_amdgcn_readlane(__builtin_bit_cast(int, alpha), 0);
      const int a1 = __builtin_amdgcn_readlane(__builtin_bit_cast(int, alpha), 2);
      if (a0 != 0x3f800000 || a1 != 0x3f800000) {
#pragma unroll
        for (int k = 0; k < 8; ++k) { acc[k] *= __builtin_bit_cast(float, a0); acc[8 + k] *= __builtin_bit_cast(float, a1); }
      }
      auto ps = __builtin_amdgcn_permlane16_swap(__float_as_uint(p), __float_as_uint(p), false, false);
      const float pl = __uint_as_float(ps[0]), ph = __uint_as_float(ps[1]);
      WAITB(V, NL);
#define PV_T(t_) do { float vf[8]; \
        _Pragma("unroll") for (int k = 0; k < 4; ++k) { vf[2 * k] = __builtin_bit_cast(float, V[t_][k] << 16); vf[2 * k + 1] = __builtin_bit_cast(float, V[t_][k] & 0xffff0000u); } \
        const float p0_ = dpp<NEWBC + ((t_) & 3) * 4>((t_) < 4 ? pl : ph), p1_ = dpp<NEWBC + ((t_) & 3) * 4 + 2>((t_) < 4 ? pl : ph); \
        _Pragma("unroll") for (int k = 0; k < 8; ++k) { acc[k] = fmaf(p0_, vf[k], acc[k]); acc[8 + k] = fmaf(p1_, vf[k], acc[8 + k]); } } while (0)
      PV_T(0); PV_T(1); PV_T(2); PV_T(3); PV_T(4); PV_T(5); PV_T(6); PV_T(7);
#undef PV_T
      ISSUE(V, vc, nphys, nmeta & 255);
      meta = nmeta;
    }
    dump(cur, PROG_DONE);
    __builtin_amdgcn_s_barrier();
    CFENCE();
    {
      constexpr int NF = W + 1;
      LAS float* rp = recs + 2 * (tid >> 7);
      LAS float* dp = recs + 8 + 2 * tid;
      f32x2 ml[NF], av[NF];
#pragma unroll
      for (int k = 0; k < NF; ++k) { ml[k] = *(LAS f32x2*)(rp + k * REC); av[k] = *(LAS f32x2*)(dp + k * REC); }
      __builtin_amdgcn_sched_barrier(0);
      float M = ml[0].x;
#pragma unroll
      for (int k = 1; k < NF; ++k) M = fmaxf(M, ml[k].x);
      float L = 0.f, o0 = 0.f, o1 = 0.f;
#pragma unroll
      for (int k = 0; k < NF; ++k) {
        const bool ok = ml[k].y > 0.f;
        const float wt = ok ? __builtin_amdgcn_exp2f(ml[k].x - M) : 0.f;
        L = fmaf(wt, ml[k].y, L); o0 = fmaf(wt, ok ? av[k].x : 0.f, o0); o1 = fmaf(wt, ok ? av[k].y : 0.f, o1);
      }
      const float inv = __builtin_amdgcn_rcpf(L);
      *op = bf16x2{(__bf16)(o0 * inv), (__bf16)(o1 * inv)};
    }
    WAITB(K, 0); WAITB(V, 0);
#undef ISSUE
#undef WAITB
  } else {
    if (lane == 0) {
      lds_st(ctl + C_READY, 0); lds_st(ctl + C_SVC, 0); lds_st(ctl + C_QRDY, 0);
      LAS float* rec = recs + RI_SVC * REC;
      rec[0] = -INFINITY; rec[1] = 0.f; rec[2] = -INFINITY; rec[3] = 0.f;
    }
    LGKM0();
    const int lraw = lens[c];
    u32x4 btr[16];
#pragma unroll
    for (int k = 0; k < 16; ++k) btr[k] = *reinterpret_cast<const u32x4*>(bt + k * 256 + lane * 4);
    const u64 ep = __hip_atomic_load((GAS u64*)(ws + WS_EPOCH) + h * B + c, RLX_AGENT);
    __builtin_amdgcn_s_barrier();
    const bool luni = lraw <= 0;
    const int llen = luni ? SKV : min(lraw, SKV);
    const int n = lane < 32 ? (llen + PAGE - 1) / PAGE : 0;
    const int P = __builtin_amdgcn_readlane(scan32(n), 31);
    const int T = max((P + B - 1) / B, R1);
    const int e = max(n - T, 0), sp = lane < 32 ? max(T - n, 0) : 0;
    const int scn = scan32(e | (sp << 16));
    const int Ein = scn & 0xffff, Sin = scn >> 16;
    const int E0 = Ein - e, S0 = Sin - sp;
    const int Etot = __builtin_amdgcn_readlane(Ein, 31);
    const int my_n = __builtin_amdgcn_readlane(n, b), my_len = __builtin_amdgcn_readlane(llen, b);
    const int hs = __builtin_amdgcn_readlane(S0, b), he = min(__builtin_amdgcn_readlane(Sin, b), Etot);
    const int nh = max(he - hs, 0);
    const int n_own = min(my_n, T);
    const int ntot = R1 + nh + max(0, n_own - R1);
    const int plo = max(E0, hs), phi = min(Ein, he);
    const u64 pmask = __ballot(lane < 32 && phi > plo);
    const int myE0 = __builtin_amdgcn_readlane(E0, b), myE1 = __builtin_amdgcn_readlane(Ein, b);
    const u64 hmask = __ballot(lane < 32 && min(min(Sin, Etot), myE1) > max(S0, myE0));
    const u64 omask = __ballot(e > 0), smask = __ballot(sp > 0), umask = __ballot(luni);
    constexpr int NP = NPOS / 64;
    int src[NP], un[NP], sg[NP], ln[NP];
#pragma unroll
    for (int k = 0; k < NP; ++k) {
      const int j = lane + 64 * k;
      src[k] = b; ln[k] = my_len;
      un[k] = j < R1 ? j : j - nh;
      sg[k] = (j < R1 || nh == 0) ? 0 : SEG_OWN2;
    }
    {
      u64 mk = pmask; int seg = 1;
      while (mk) {
        const int bp = __builtin_ctzll(mk); mk &= mk - 1;
        const int lo = __builtin_amdgcn_readlane(plo, bp), hi = __builtin_amdgcn_readlane(phi, bp);
        const int e0 = __builtin_amdgcn_readlane(E0, bp), bl = __builtin_amdgcn_readlane(llen, bp);
#pragma unroll
        for (int k = 0; k < NP; ++k) {
          const int xq = lane + 64 * k - R1 + hs;
          const bool in = xq >= lo && xq < hi;
          src[k] = in ? bp : src[k]; un[k] = in ? T + xq - e0 : un[k]; sg[k] = in ? seg : sg[k]; ln[k] = in ? bl : ln[k];
        }
        ++seg;
      }
    }
#pragma unroll
    for (int k = 0; k < 16; ++k) *(LAS u32x4*)(sm + L_BT + k * 1024 + lane * 16) = btr[k];
    LGKM0();
#pragma unroll
    for (int k = 0; k < NP; ++k) {
      const int j = lane + 64 * k;
      const int phys = *(LAS int*)(sm + L_BT + (src[k] * NBPS + min(un[k], NBPS - 1)) * 4);
      const int rem = j < ntot ? max(0, min(PAGE, ln[k] - un[k] * PAGE)) : 0;
      *(LAS i32x2*)(sm + L_LIST + j * 8) = i32x2{phys, rem | (sg[k] << 8)};
    }
    if (lane == 0) lds_st(ctl + C_NTOT, ntot);
    LGKM0();
    if (lane == 0) lds_st(ctl + C_READY, 1);

    const u64 tagv = ep * 6364136223846793005ull + 1442695040888963407ull;
    if (pmask) {
      {
        const u32x4 z = {0u, 0u, 0u, 0u};
        const u32x4 q0 = *reinterpret_cast<const u32x4*>(q + (size_t)(b * HQ + h * 2) * D + 8 * lane);
        u64 mk = pmask;
        u32x4 qq[3]; int bb[3];
#pragma unroll
        for (int k = 0; k < 3; ++k) {
          bb[k] = mk ? __builtin_ctzll(mk) : b; mk &= mk - 1;
          qq[k] = *reinterpret_cast<const u32x4*>(q + (size_t)(bb[k] * HQ + h * 2) * D + 8 * lane);
        }
        *(LAS u32x4*)(sm + L_Q + lane * 16) = ((umask >> b) & 1) ? z : q0;
#pragma unroll
        for (int k = 0; k < 3; ++k) *(LAS u32x4*)(sm + L_Q + (k + 1) * 1024 + lane * 16) = ((umask >> bb[k]) & 1) ? z : qq[k];
        int seg = 4;
        while (mk) {
          const int bp = __builtin_ctzll(mk); mk &= mk - 1;
          const u32x4 qx = *reinterpret_cast<const u32x4*>(q + (size_t)(bp * HQ + h * 2) * D + 8 * lane);
          *(LAS u32x4*)(sm + L_Q + seg * 1024 + lane * 16) = ((umask >> bp) & 1) ? z : qx;
          ++seg;
        }
        LGKM0();
        if (lane == 0) lds_st(ctl + C_QRDY, 1);
      }
      u64 mk = pmask; int seg = 1;
      while (mk) {
        const int bp = __builtin_ctzll(mk); mk &= mk - 1;
        for (;;) {
          const int pr = lane < W ? lds_ld(ctl + C_PROG + lane) : PROG_DONE;
          if (__all(pr > seg)) break;
          __builtin_amdgcn_s_sleep(4);
        }
        CFENCE();
        LAS float* rb = recs + (RI_RING + ((seg - 1) % NSLOT) * W) * REC;
        float M = -INFINITY, mw[W], lw[W];
#pragma unroll
        for (int k = 0; k < W; ++k) {
          const int tg = ((LAS int*)(rb + k * REC))[4];
          const f32x2 ml = *(LAS f32x2*)(rb + k * REC + 2 * r);
          mw[k] = tg == seg ? ml.x : -INFINITY; lw[k] = tg == seg ? ml.y : 0.f;
          M = fmaxf(M, mw[k]);
        }
        float L = 0.f; f32x4 o0 = {0.f, 0.f, 0.f, 0.f}, o1 = o0;
#pragma unroll
        for (int k = 0; k < W; ++k) {
          const bool ok = lw[k] > 0.f;
          const float wt = ok ? __builtin_amdgcn_exp2f(mw[k] - M) : 0.f;
          f32x4 a0 = *(LAS f32x4*)(rb + k * REC + 8 + lane * 8), a1 = *(LAS f32x4*)(rb + k * REC + 8 + lane * 8 + 4);
          const f32x4 z = {0.f, 0.f, 0.f, 0.f};
          a0 = ok ? a0 : z; a1 = ok ? a1 : z;
          L = fmaf(wt, lw[k], L); o0 += wt * a0; o1 += wt * a1;
        }
        asm volatile("s_waitcnt lgkmcnt(0)" : "+v"(o0), "+v"(o1) :: "memory");
        if (lane == 0) lds_st(ctl + C_SVC, seg);
        const int pid = h * 64 + __builtin_popcountll(omask & ((1ull << bp) - 1)) + __builtin_popcountll(smask & ((1ull << b) - 1));
        const __amdgpu_buffer_rsrc_t rs = __builtin_amdgcn_make_buffer_rsrc((void*)(ws + WS_PAY + (size_t)pid * PAY_STRIDE), 0, PAY_STRIDE, 0x00020000);
        __builtin_amdgcn_raw_buffer_store_b128(__builtin_bit_cast(u32x4, o0), rs, 128 + lane * 32, 0, 16);
        __builtin_amdgcn_raw_buffer_store_b128(__builtin_bit_cast(u32x4, o1), rs, 128 + lane * 32 + 16, 0, 16);
        if (c == 0) __builtin_amdgcn_raw_buffer_store_b64(__builtin_bit_cast(u32x2, f32x2{M, L}), rs, r * 8, 0, 16);
        VM0();
        const u64 tag = readlane64(tagv, bp);
        if (lane == 0) __hip_atomic_store((GAS u64*)(ws + WS_FLAG) + (h * B + bp) * B + b, tag, RLX_AGENT);
        ++seg;
      }
    }
    if (hmask) {
      const u64 tag = readlane64(tagv, b);
      const bool mine = (hmask >> lane) & 1;
      for (unsigned spins = 0; spins < (1u << 18); ++spins) {
        const u64 f = __hip_atomic_load((GAS u64*)(ws + WS_FLAG) + (h * B + b) * B + c, RLX_AGENT);
        if (__all(!mine || f == tag)) break;
        __builtin_amdgcn_s_sleep(16);
      }
      __builtin_amdgcn_fence(__ATOMIC_ACQUIRE, "agent");
      float M = -INFINITY, L = 0.f; f32x4 o0 = {0.f, 0.f, 0.f, 0.f}, o1 = o0;
      u64 mk = hmask;
      while (mk) {
        const int hb = __builtin_ctzll(mk); mk &= mk - 1;
        const int pid = h * 64 + __builtin_popcountll(omask & ((1ull << b) - 1)) + __builtin_popcountll(smask & ((1ull << hb) - 1));
        const __amdgpu_buffer_rsrc_t rs = __builtin_amdgcn_make_buffer_rsrc((void*)(ws + WS_PAY + (size_t)pid * PAY_STRIDE), 0, PAY_STRIDE, 0x00020000);
        const f32x4 a0 = __builtin_bit_cast(f32x4, __builtin_amdgcn_raw_buffer_load_b128(rs, 128 + lane * 32, 0, 16));
        const f32x4 a1 = __builtin_bit_cast(f32x4, __builtin_amdgcn_raw_buffer_load_b128(rs, 128 + lane * 32 + 16, 0, 16));
        const f32x2 ml = __builtin_bit_cast(f32x2, __builtin_amdgcn_raw_buffer_load_b64(rs, r * 8, 0, 16));
        const float Mn = fmaxf(M, ml.x);
        const float wo = (L > 0.f) ? __builtin_amdgcn_exp2f(M - Mn) : 0.f, wn = (ml.y > 0.f) ? __builtin_amdgcn_exp2f(ml.x - Mn) : 0.f;
        L = L * wo + ml.y * wn; o0 = o0 * wo + a0 * wn; o1 = o1 * wo + a1 * wn; M = Mn;
      }
      LAS float* rec = recs + RI_SVC * REC;
      *(LAS f32x4*)(rec + 8 + lane * 8) = o0;
      *(LAS f32x4*)(rec + 8 + lane * 8 + 4) = o1;
      if (c == 0) *(LAS f32x2*)(rec + 2 * r) = f32x2{M, L};
      if (lane == 0) __hip_atomic_store((GAS u64*)(ws + WS_EPOCH) + h * B + b, tag, RLX_AGENT);
    }
    LGKM0();
    __builtin_amdgcn_s_barrier();
  }
}

extern "C" void kernel_launch(void* const* d_in, const int* in_sizes, int n_in, void* d_out, int out_size, void* d_ws,
                              size_t ws_size, hipStream_t stream) {
  attn_kernel<<<B * HKV, NTHR, 0, stream>>>((const uint16_t*)d_in[0], (const char*)d_in[1], (const char*)d_in[2],
                                            (const int*)d_in[3], (const int*)d_in[4], (uint16_t*)d_out, (char*)d_ws);
}
```
